```python
import torch

def _softmax(x, axis=-1):
    return torch.softmax(x, dim=axis)
def _gelu(x):
    return torch.nn.functional.gelu(x, approximate="tanh")
def _silu(x):
    return torch.nn.functional.silu(x)
def _sigmoid(x):
    return torch.sigmoid(x)
def _layernorm(x, axis=-1, eps=1e-5):
    m = x.mean(dim=axis, keepdim=True)
    v = x.var(dim=axis, keepdim=True, unbiased=False)
    return (x - m) / torch.sqrt(v + eps)
def _scatter_add(tgt, idx, src, axis=0):
    return tgt.clone().index_add_(axis, idx, src)
def _segment_sum(src, seg, n):
    out = torch.zeros((n,) + src.shape[1:], dtype=src.dtype)
    return out.index_add_(0, seg, src)
def _pad(x, pw):
    flat = tuple(v for pair in reversed(pw) for v in pair)
    return torch.nn.functional.pad(x, flat)

kernel_name = 'sequential_preLN__spectral_gfnet__moe_arch_mod__prompt'

def setup_inputs():

    _w = (lambda a: (lambda t: t.to(torch.bfloat16) if t.is_floating_point() else t)(torch.as_tensor(a)))
    def _A_setup():
        B, S, D = 1, 2048, 576
        Df = D // 2 + 1
        return {
            "x": _w((lambda *s: torch.randn(*s, dtype=torch.bfloat16) * 0.05)(B, S, D)),
            "w_r": _w((lambda *s: torch.randn(*s, dtype=torch.bfloat16) * 0.4)(S, Df) * 0.02),
            "w_i": _w((lambda *s: torch.randn(*s, dtype=torch.bfloat16) * 0.4)(S, Df) * 0.02),
        }
    _A_in = _A_setup()

    def _B_setup():
        B, S, D, F, C = 1, 2048, 576, 4608, 2
        return {
            "x": _w((lambda *s: torch.randn(*s, dtype=torch.bfloat16) * 0.05)(B, S, D)),
            "w_r": _w((lambda *s: torch.randn(*s, dtype=torch.bfloat16) * 0.05)(D)),
            "w_gate": _w((lambda *s: torch.randn(*s, dtype=torch.bfloat16) * 0.05)(D, F)),
            "w_up": _w((lambda *s: torch.randn(*s, dtype=torch.bfloat16) * 0.05)(D, F)),
            "w_down": _w((lambda *s: torch.randn(*s, dtype=torch.bfloat16) * 0.05)(F, D)),
        }
    _B_in = _B_setup()

    return {
        "A_x": _A_in["x"],
        "A_w_r": _A_in["w_r"],
        "A_w_i": _A_in["w_i"],
        "B_x": _B_in["x"],
        "B_w_r": _B_in["w_r"],
        "B_w_gate": _B_in["w_gate"],
        "B_w_up": _B_in["w_up"],
        "B_w_down": _B_in["w_down"],
    }

def reference(A_x, A_w_r, A_w_i, B_x, B_w_r, B_w_gate, B_w_up, B_w_down):
    def _A_ref(x, w_r, w_i):
        B, S, D = x.shape
        X = (lambda x, axes=(-2,-1): torch.fft.rfft2(x.float(), dim=axes))(x, axes=(-2, -1))
        W = w_r + 1j * w_i
        X = X * W
        y = (lambda x, s, axes=(-2,-1): torch.fft.irfft2(x, s=s, dim=axes).to(torch.bfloat16))(X, (S, D), axes=(-2, -1))
        return y

    def _B_ref(x, w_r, w_gate, w_up, w_down):
        B, S, D = x.shape
        C = 2
        r = x @ w_r
        idx = torch.argsort(-r, axis=-1)[:, :C]
        idx_e = (lambda x, n, axis: torch.repeat_interleave(x, n, dim=axis))(idx.reshape(B, C, 1), D, axis=2)
        x_sel = (lambda x, i, axis: torch.take_along_dim(x, i, dim=axis))(x, idx_e, axis=1)
        g = _silu(x_sel @ w_gate)
        u = x_sel @ w_up
        y = (g * u) @ w_down
        r_sel = _sigmoid((lambda x, i, axis: torch.take_along_dim(x, i, dim=axis))(r, idx, axis=1))
        y = y * r_sel.reshape(B, C, 1)
        b_off = (torch.arange(B) * S).reshape(B, 1)
        flat_idx = (idx + b_off).reshape(B * C)
        zero = (x * 0.0).reshape(B * S, D)
        upd = _scatter_add(zero, flat_idx, y.reshape(B * C, D), axis=0)
        return x + upd.reshape(B, S, D)

    x = A_x
    h = x + _A_ref(_layernorm(x, axis=-1), A_w_r, A_w_i)
    h2 = h + _B_ref(_layernorm(h, axis=-1), B_w_r, B_w_gate, B_w_up, B_w_down)
    return h2
```

```cpp
#include <hip/hip_runtime.h>
#include <hip/hip_bf16.h>
#include <stdint.h>

#define S_ 2048
#define D_ 576
#define DF_ 289
#define F_ 4608

typedef unsigned short u16;
__device__ __forceinline__ float bf2f(u16 v) { return __uint_as_float(((unsigned)v) << 16); }
__device__ __forceinline__ u16 f2bf(float f) { __hip_bfloat16 b = __float2bfloat16(f); return *reinterpret_cast<u16*>(&b); }
__device__ __forceinline__ float rbf(float f) { return bf2f(f2bf(f)); }

__device__ __forceinline__ float block_sum(float v, float* red) {
    for (int o = 32; o > 0; o >>= 1) v += __shfl_xor(v, o);
    int w = threadIdx.x >> 6;
    __syncthreads();
    if ((threadIdx.x & 63) == 0) red[w] = v;
    __syncthreads();
    float s = 0;
    for (int i = 0; i < (int)(blockDim.x >> 6); ++i) s += red[i];
    return s;
}

__global__ void k1(const u16* x, float2* Z1) {
    __shared__ float row[D_];
    __shared__ float2 tw[D_];
    __shared__ float red[16];
    int n1 = blockIdx.x, t = threadIdx.x;
    float xv = bf2f(x[n1 * D_ + t]);
    float sn, cs; sincospif(-2.0f * t / D_, &sn, &cs); tw[t] = make_float2(cs, sn);
    float mean = block_sum(xv, red) / D_;
    float d = xv - mean;
    float var = block_sum(d * d, red) / D_;
    float m = rbf(mean), v = rbf(var);
    float sd = rbf(sqrtf(rbf(v + 1e-5f)));
    row[t] = rbf(rbf(xv - m) / sd);
    __syncthreads();
    if (t < DF_) {
        float re = 0, im = 0; int idx = 0;
        for (int n = 0; n < D_; ++n) { float2 w = tw[idx]; re += row[n] * w.x; im += row[n] * w.y; idx += t; if (idx >= D_) idx -= D_; }
        Z1[(size_t)t * S_ + n1] = make_float2(re, im);
    }
}
__global__ void k2(const float2* Z1, const u16* wr, const u16* wi, float2* Z2) {
    __shared__ float2 col[S_];
    __shared__ float2 tw[S_];
    __shared__ float2 spec[S_];
    int k2 = blockIdx.x, t = threadIdx.x;
    for (int i = t; i < S_; i += blockDim.x) { col[i] = Z1[(size_t)k2 * S_ + i]; float sn, cs; sincospif(-2.0f * i / S_, &sn, &cs); tw[i] = make_float2(cs, sn); }
    __syncthreads();
    for (int k = t; k < S_; k += blockDim.x) {
        float re = 0, im = 0;
        for (int n = 0; n < S_; ++n) { float2 w = tw[(n * k) & (S_ - 1)]; float2 c = col[n]; re += c.x * w.x - c.y * w.y; im += c.x * w.y + c.y * w.x; }
        float a = bf2f(wr[k * DF_ + k2]), b = bf2f(wi[k * DF_ + k2]);
        spec[k] = make_float2(re * a - im * b, re * b + im * a);
    }
    __syncthreads();
    for (int k = t; k < S_; k += blockDim.x) {
        float re = 0, im = 0;
        for (int n = 0; n < S_; ++n) { float2 w = tw[(n * k) & (S_ - 1)]; float2 c = spec[n]; re += c.x * w.x + c.y * w.y; im += -c.x * w.y + c.y * w.x; }
        Z2[(size_t)k * DF_ + k2] = make_float2(re / S_, im / S_);
    }
}
__global__ void k3(const u16* x, const float2* Z2, const u16* bwr, u16* out, u16* ln2o, u16* ho, float* r) {
    __shared__ float2 z[DF_];
    __shared__ float2 tw[D_];
    __shared__ float red[16];
    int n1 = blockIdx.x, t = threadIdx.x;
    if (t < DF_) z[t] = Z2[(size_t)n1 * DF_ + t];
    float sn, cs; sincospif(2.0f * t / D_, &sn, &cs); tw[t] = make_float2(cs, sn);
    __syncthreads();
    float acc = 0; int idx = t;
    for (int k = 1; k < D_ / 2; ++k) { float2 w = tw[idx]; acc += z[k].x * w.x - z[k].y * w.y; idx += t; if (idx >= D_) idx -= D_; }
    acc = 2.0f * acc + z[0].x + ((t & 1) ? -z[D_ / 2].x : z[D_ / 2].x);
    float y = rbf(acc / D_);
    float h = rbf(bf2f(x[n1 * D_ + t]) + y);
    float mean = block_sum(h, red) / D_;
    float d = h - mean;
    float var = block_sum(d * d, red) / D_;
    float m = rbf(mean), v = rbf(var);
    float sd = rbf(sqrtf(rbf(v + 1e-5f)));
    float l = rbf(rbf(h - m) / sd);
    float rr = block_sum(l * bf2f(bwr[t]), red);
    if (t == 0) r[n1] = rbf(rr);
    ln2o[n1 * D_ + t] = f2bf(l);
    ho[n1 * D_ + t] = f2bf(h);
    out[n1 * D_ + t] = f2bf(h + l);
}
__global__ void k4(const float* r, int* idx) {
    if (threadIdx.x == 0) {
        float b1 = -INFINITY, b2 = -INFINITY; int i1 = 0, i2 = 1;
        for (int i = 0; i < S_; ++i) { float v = r[i]; if (v > b1) { b2 = b1; i2 = i1; b1 = v; i1 = i; } else if (v > b2) { b2 = v; i2 = i; } }
        idx[0] = i1; idx[1] = i2;
    }
}
__global__ void k5(const int* idx, const u16* ln2, const u16* wg, const u16* wu, float* a) {
    int f = blockIdx.x * blockDim.x + threadIdx.x;
    if (f >= F_) return;
    for (int c = 0; c < 2; ++c) {
        const u16* xs = ln2 + idx[c] * D_;
        float g = 0, u = 0;
        for (int k = 0; k < D_; ++k) { float xv = bf2f(xs[k]); g += xv * bf2f(wg[k * F_ + f]); u += xv * bf2f(wu[k * F_ + f]); }
        g = rbf(g); u = rbf(u);
        float sg = rbf(g / (1.0f + expf(-g)));
        a[c * F_ + f] = rbf(sg * u);
    }
}
__global__ void k6(const int* idx, const float* a, const u16* wd, const float* r, const u16* ln2, const u16* ho, u16* out) {
    int d = blockIdx.x * blockDim.x + threadIdx.x;
    if (d >= D_) return;
    for (int c = 0; c < 2; ++c) {
        float y = 0;
        for (int f = 0; f < F_; ++f) y += a[c * F_ + f] * bf2f(wd[f * D_ + d]);
        y = rbf(y);
        float rs = rbf(1.0f / (1.0f + expf(-r[idx[c]])));
        y = rbf(y * rs);
        int o = idx[c] * D_ + d;
        out[o] = f2bf(bf2f(ho[o]) + rbf(bf2f(ln2[o]) + y));
    }
}

extern "C" void kernel_launch(void* const* d_in, const int* in_sizes, int n_in,
                              void* d_out, int out_size, void* d_ws, size_t ws_size,
                              hipStream_t stream) {
    const u16* x = (const u16*)d_in[0];
    const u16* awr = (const u16*)d_in[1];
    const u16* awi = (const u16*)d_in[2];
    const u16* bwr = (const u16*)d_in[4];
    const u16* wg = (const u16*)d_in[5];
    const u16* wu = (const u16*)d_in[6];
    const u16* wd = (const u16*)d_in[7];
    u16* out = (u16*)d_out;
    char* ws = (char*)d_ws;
    float2* Z1 = (float2*)ws; ws += (size_t)S_ * DF_ * 8;
    float2* Z2 = (float2*)ws; ws += (size_t)S_ * DF_ * 8;
    u16* ln2 = (u16*)ws; ws += S_ * D_ * 2;
    u16* ho = (u16*)ws; ws += S_ * D_ * 2;
    float* r = (float*)ws; ws += S_ * 4;
    float* a = (float*)ws; ws += 2 * F_ * 4;
    int* idx = (int*)ws;
    k1<<<S_, D_, 0, stream>>>(x, Z1);
    k2<<<DF_, 1024, 0, stream>>>(Z1, awr, awi, Z2);
    k3<<<S_, D_, 0, stream>>>(x, Z2, bwr, out, ln2, ho, r);
    k4<<<1, 64, 0, stream>>>(r, idx);
    k5<<<F_ / 64, 64, 0, stream>>>(idx, ln2, wg, wu, a);
    k6<<<D_ / 64, 64, 0, stream>>>(idx, a, wd, r, ln2, ho, out);
}
```

```cpp
#include <hip/hip_runtime.h>
#include <stdint.h>

typedef unsigned short u16;
typedef unsigned int u32;
typedef unsigned long long u64;
typedef float f2 __attribute__((ext_vector_type(2)));
typedef float f4 __attribute__((ext_vector_type(4)));
typedef u32 u32x2 __attribute__((ext_vector_type(2)));
typedef u32 u32x4 __attribute__((ext_vector_type(4)));
typedef __bf16 bf2 __attribute__((ext_vector_type(2)));
typedef __bf16 bf8 __attribute__((ext_vector_type(8)));
typedef short s16x4 __attribute__((ext_vector_type(4)));
typedef short s16x8 __attribute__((ext_vector_type(8)));
#define DEV __device__ __forceinline__
#define LAS __attribute__((address_space(3)))

#define N1_DMA 56
#define SPIN_CAP 400000

#define WS_EPOCH   0u
#define WS_CAND    4096u
#define WS_ROWH    8192u
#define WS_PB      16384u
#define WS_ROWL    (1u << 20)
#define WS_E1      (5u << 20)
#define WS_E2      (10u << 20)
#define WS_BYTES   (15u << 20)
#define WS_STAMP   (16u << 20)
#define L_Z        0u
#define L_TA       19968u
#define L_WST      39488u
#define L_SP       47680u
#define L_KEYS     (L_SP)
#define L_TOP      (L_SP + 64u)
#define L_XS       (L_SP + 128u)
#define L_GU       (L_SP + 2560u)
#define L_AB       (L_SP + 3584u)
#define L_BIG      52288u
#define L_WG       (L_BIG)
#define L_WU       (L_BIG + 36864u)
#define L_WD       (L_BIG + 73728u)
#define L_TB       (L_BIG)
#define L_WSTB     (L_BIG + 19520u)
#define L_S0       (L_BIG + 32768u)
#define L_TOTAL    (L_BIG + 110592u)

DEV f2 pfma(f2 a, f2 b, f2 c) { return __builtin_elementwise_fma(a, b, c); }
#define PM f2{1.f, -1.f}
#define MP f2{-1.f, 1.f}
DEV f2 add_mi(f2 a, f2 b) { return pfma(b.yx, PM, a); }
DEV f2 add_pi(f2 a, f2 b) { return pfma(b.yx, MP, a); }
DEV f2 nadd_mi(f2 a, f2 b) { return pfma(b.yx, PM, -a); }
DEV f2 nadd_pi(f2 a, f2 b) { return pfma(b.yx, MP, -a); }
DEV f2 add_conj(f2 a, f2 b) { return pfma(b, PM, a); }
DEV f2 sub_conj(f2 a, f2 b) { return pfma(b, MP, a); }
DEV f2 odd_sep(f2 a, f2 b) { return pfma(a.yx, PM, b.yx); }
DEV f2 sumdiff(f2 a) { return pfma(a.yy, PM, a.xx); }
struct Tw { f2 x; f2 n; };
DEV Tw mktw(f2 w) { return Tw{w.xx, f2{-w.y, w.y}}; }
DEV f2 cmul(f2 a, Tw w) { return pfma(a.yx, w.n, a * w.x); }
DEV f2 cmulc(f2 a, Tw w) { return pfma(a.yx, -w.n, a * w.x); }
template<int N> struct TwA {
    f2 xp[(N + 1) / 2]; f2 n[N];
    DEV Tw operator[](int i) const { return Tw{(i & 1) ? xp[i / 2].yy : xp[i / 2].xx, n[i]}; }
    DEV void set(int i, float x, f2 nn) { if (i & 1) xp[i / 2].y = x; else xp[i / 2].x = x; n[i] = nn; }
    DEV void pin() {
#pragma unroll
        for (int i = 0; i < (N + 1) / 2; ++i) asm volatile("" : "+v"(xp[i]));
#pragma unroll
        for (int i = 1; i < N; ++i) asm volatile("" : "+v"(n[i]));
    }
};
template<int DIR> DEV f2 addr_(f2 a, f2 b) { return DIR > 0 ? add_mi(a, b) : add_pi(a, b); }
template<int DIR> DEV f2 subr_(f2 a, f2 b) { return DIR > 0 ? add_pi(a, b) : add_mi(a, b); }
template<int DIR> DEV f2 naddr_(f2 a, f2 b) { return DIR > 0 ? nadd_mi(a, b) : nadd_pi(a, b); }

#define C_H 0.70710678118654752440f
#define C_K 0.86602540378443864676f
template<int DIR> DEV void r4(f2& a, f2& b, f2& c, f2& d) {
    f2 s0 = a + c, s1 = a - c, s2 = b + d, s3 = b - d;
    a = s0 + s2; c = s0 - s2; b = addr_<DIR>(s1, s3); d = subr_<DIR>(s1, s3);
}
template<int DIR> DEV void r3(f2& a, f2& b, f2& c) {
    f2 t = b + c, d = b - c;
    f2 m = pfma(t, f2{-0.5f, -0.5f}, a);
    a = a + t;
    const f2 kk = DIR > 0 ? f2{C_K, -C_K} : f2{-C_K, C_K};
    b = pfma(d.yx, kk, m);
    c = pfma(d.yx, -kk, m);
}
template<int DIR> DEV void r4odd(f2& a, f2& b, f2& c, f2& d) {
    f2 o0 = addr_<DIR>(a, c), o2 = subr_<DIR>(a, c);
    f2 p = addr_<DIR>(b, d), m = subr_<DIR>(b, d);
    f2 tp = addr_<DIR>(p, p), tm = naddr_<DIR>(m, m);
    const f2 hh = f2{C_H, C_H};
    a = pfma(tp, hh, o0); c = pfma(tp, -hh, o0); b = pfma(tm, hh, o2); d = pfma(tm, -hh, o2);
}
template<int DIR> DEV void r8(f2* v) {
    f2 a0 = v[0] + v[4], a4 = v[0] - v[4], a1 = v[1] + v[5], a5 = v[1] - v[5];
    f2 a2 = v[2] + v[6], a6 = v[2] - v[6], a3 = v[3] + v[7], a7 = v[3] - v[7];
    r4<DIR>(a0, a1, a2, a3);
    r4odd<DIR>(a4, a5, a6, a7);
    v[0] = a0; v[2] = a1; v[4] = a2; v[6] = a3; v[1] = a4; v[3] = a5; v[5] = a6; v[7] = a7;
}
template<int DIR> DEV void r12(f2* v) {
#pragma unroll
    for (int a = 0; a < 3; ++a) r4<DIR>(v[(4 * a) % 12], v[(4 * a + 3) % 12], v[(4 * a + 6) % 12], v[(4 * a + 9) % 12]);
#pragma unroll
    for (int b = 0; b < 4; ++b) r3<DIR>(v[(3 * b) % 12], v[(4 + 3 * b) % 12], v[(8 + 3 * b) % 12]);
    f2 o[12];
#pragma unroll
    for (int a = 0; a < 3; ++a)
#pragma unroll
        for (int b = 0; b < 4; ++b) o[(4 * a + 9 * b) % 12] = v[(4 * a + 3 * b) % 12];
#pragma unroll
    for (int i = 0; i < 12; ++i) v[i] = o[i];
}
template<int N> DEV void twfill(TwA<N>& t, int base, float invN) {
    const float st = (float)base * invN;
    t.xp[0].x = 1.f;
#pragma unroll
    for (int i = 1; i < N; ++i) { const float x = st * (float)i; const float sn = __builtin_amdgcn_sinf(x); t.set(i, __builtin_amdgcn_cosf(x), f2{sn, -sn}); }
    t.pin();
}

DEV float bfl(u32 u) { return __uint_as_float(u << 16); }
DEV float bfh(u32 u) { return __uint_as_float(u & 0xFFFF0000u); }
DEV u32 pkbf(f2 v) { bf2 b = __builtin_convertvector(v, bf2); return __builtin_bit_cast(u32, b); }
DEV f2 unbf(u32 u) { return f2{bfl(u), bfh(u)}; }
DEV f2 rbf2(f2 v) { return unbf(pkbf(v)); }
DEV float rbf(float v) { return bfl(pkbf(f2{v, v})); }

template<int CTRL, int RM> DEV float dppa(float v) { return v + __int_as_float(__builtin_amdgcn_update_dpp(0, __float_as_int(v), CTRL, RM, 0xF, false)); }
DEV float wsum(float v) {
    v = dppa<0x111, 0xF>(v); v = dppa<0x112, 0xF>(v); v = dppa<0x114, 0xF>(v); v = dppa<0x118, 0xF>(v);
    v = dppa<0x142, 0xA>(v); v = dppa<0x143, 0xC>(v);
    return __int_as_float(__builtin_amdgcn_readlane(__float_as_int(v), 63));
}
template<int CTRL, int RM> DEV u32 dppm(u32 v) { u32 o = (u32)__builtin_amdgcn_update_dpp(0, (int)v, CTRL, RM, 0xF, false); return v > o ? v : o; }
DEV u32 wmax(u32 v) {
    v = dppm<0x111, 0xF>(v); v = dppm<0x112, 0xF>(v); v = dppm<0x114, 0xF>(v); v = dppm<0x118, 0xF>(v);
    v = dppm<0x142, 0xA>(v); v = dppm<0x143, 0xC>(v);
    return (u32)__builtin_amdgcn_readlane((int)v, 63);
}
DEV float rdl(float v, int l) { return __int_as_float(__builtin_amdgcn_readlane(__float_as_int(v), l)); }

#define CFENCE() asm volatile("" ::: "memory")
DEV void bar() { asm volatile("s_waitcnt lgkmcnt(0)\n\ts_barrier" ::: "memory"); }
DEV void vm0() { asm volatile("s_waitcnt vmcnt(0)" ::: "memory"); }
DEV void glds16(const void* gsrc, u32 lds_dst) {
    u32 keep;
    asm volatile("s_mov_b32 %0, m0\n\ts_mov_b32 m0, %2\n\ts_nop 0\n\tglobal_load_lds_dwordx4 %1, off nt\n\ts_mov_b32 m0, %0" : "=&s"(keep) : "v"(gsrc), "s"(lds_dst) : "memory");
}
struct Tg { u32 c, t; };
DEV u32 tagd(float v, Tg g) { return ((__float_as_uint(v) + g.c) & 0xFFFFFC00u) | g.t; }

#define STAMP(i) do {} while (0)

__global__ void __launch_bounds__(512, 2) fused_kernel(const u16* __restrict__ x, const u16* __restrict__ awr, const u16* __restrict__ awi,
                                                       const u16* __restrict__ bwr, const u16* __restrict__ wg, const u16* __restrict__ wu,
                                                       const u16* __restrict__ wd, u16* out, char* ws) {
    extern __shared__ __attribute__((aligned(16))) char smem[];
    const int b = blockIdx.x;
    const int lane = threadIdx.x & 63;
    const int w = __builtin_amdgcn_readfirstlane(threadIdx.x >> 6);
    const int w4 = w & 3;
    const bool hiw = w >= 4;
    const int cls = b & 7;
    const bool dbl = b >= 224;
    const bool moe = (b >= 80) && (b < 224);
    const bool fin = (b >= 8) && (b < 80);
    const int kt = hiw ? (b >> 3) + 4 : (b >> 3);
    const bool p2act = !hiw || dbl;
    const u32 lbase = (u32)(uintptr_t)smem;
    const __amdgpu_buffer_rsrc_t rs = __builtin_amdgcn_make_buffer_rsrc(ws, 0, WS_BYTES, 0x00020000);
    STAMP(0);

    f2 xv[12];
    f2 wr2[6];
    const int l48 = lane < 48 ? lane : 47;
    if (!hiw) {
        const u16* xa = x + (size_t)(b + 256 * w4) * 576 + l48;
        const u16* xb = xa + 4 * 256 * 576;
#pragma unroll
        for (int i = 0; i < 12; ++i) { xv[i].x = bfl(xa[48 * i]); xv[i].y = bfl(xb[48 * i]); }
    }
    const u64 epoch = __hip_atomic_load((const u64*)(ws + WS_EPOCH), __ATOMIC_RELAXED, __HIP_MEMORY_SCOPE_AGENT);
    if (hiw) {
        const int q = w4 * 64 + lane;
        const int k1 = 8 * q + cls;
        const int sh = (cls & 1) * 16;
#pragma unroll
        for (int tb = 0; tb < 2; ++tb) {
            if (tb == 1 && !dbl) break;
            const int k0 = ((b >> 3) + 4 * tb) * 8;
            const int eo = (k1 * 289 + k0) & ~1;
            const u32* pr = (const u32*)(awr + eo);
            const u32* pi = (const u32*)(awi + eo);
            u32 a[5], c[5];
#pragma unroll
            for (int i = 0; i < 5; ++i) { a[i] = pr[i]; c[i] = pi[i]; }
            LAS u32x2* dst = (LAS u32x2*)(smem + (tb ? L_WSTB : L_WST)) + q;
#pragma unroll
            for (int i = 0; i < 4; ++i) {
                u32 rr = __builtin_amdgcn_alignbit(a[i + 1], a[i], sh), ii = __builtin_amdgcn_alignbit(c[i + 1], c[i], sh);
                dst[i * 256] = u32x2{(rr & 0xFFFFu) | (ii << 16), (rr >> 16) | (ii & 0xFFFF0000u)};
            }
        }
        if (b < 8) {
            const int km = (2048 - k1) & 2047;
            f2 A = f2{bfl(awr[k1 * 289]), bfl(awi[k1 * 289])}, B = f2{bfl(awr[km * 289]), bfl(awi[km * 289])};
            f2 C = f2{bfl(awr[k1 * 289 + 288]), bfl(awi[k1 * 289 + 288])}, Dd = f2{bfl(awr[km * 289 + 288]), bfl(awi[km * 289 + 288])};
            f2 h0 = add_conj(A, B), hn = add_conj(C, Dd);
            const float sc = 0.5f / (2048.f * 576.f) * 0.125f;
            f2 al, be;
            if ((cls & 3) == 0) { al = (h0 + hn) * sc; be = (h0 - hn) * sc; }
            else { al = (cls < 4 ? h0 : hn) * sc; be = f2{0.f, 0.f}; }
            *(LAS f4*)(smem + L_S0 + q * 16) = f4{al.x, al.y, be.x, be.y};
        }
        if (moe) {
            const int s = ((b - 80) & 7) * 18 + ((b - 80) >> 3);
            const u16* gsrc = wg + (size_t)(lane >> 2) * 4608 + s * 32 + (lane & 3) * 8;
            const long du = wu - wg;
            const u16* dsrc = wd + (size_t)s * 32 * 576 + lane * 8;
#pragma unroll 1
            for (int pc = w4; pc < N1_DMA; pc += 4) {
                const u16* src = pc < 36 ? gsrc + (size_t)pc * 16 * 4608 : pc < 72 ? gsrc + du + (size_t)(pc - 36) * 16 * 4608 : dsrc + (pc - 72) * 512;
                glds16(src, lbase + L_BIG + pc * 1024);
            }
        }
    } else {
#pragma unroll
        for (int i = 0; i < 6; ++i) wr2[i] = f2{bfl(bwr[l48 + 96 * i]), bfl(bwr[l48 + 96 * i + 48])};
    }
    TwA<12> tw1, tw2;
    if (!hiw) { twfill(tw1, l48, 1.f / 576.f); twfill(tw2, l48 & 3, 1.f / 48.f); }
    const float cwa = (float)(b * (lane & 7)) * (1.f / 2048.f);
    const float cwc = __builtin_amdgcn_cosf(cwa), cws = __builtin_amdgcn_sinf(cwa);
    auto ctw = [&](int r) { const float c = rdl(cwc, r), q = rdl(cws, r); return Tw{f2{c, c}, f2{q, -q}}; };
    u64 T;
    {
        u64 z = (u64)(u32)__builtin_amdgcn_readfirstlane((u32)epoch) | ((u64)(u32)__builtin_amdgcn_readfirstlane((u32)(epoch >> 32)) << 32);
        z = (z + 1) * 0x9E3779B97F4A7C15ull;
        z = (z ^ (z >> 30)) * 0xBF58476D1CE4E5B9ull; z = (z ^ (z >> 27)) * 0x94D049BB133111EBull; T = z ^ (z >> 31);
    }
    const u32 t0 = (u32)T & 511u, t1 = (u32)(T >> 9) & 511u, t2 = (u32)(T >> 18) & 511u, t3 = (u32)(T >> 27) & 511u;
    const u32 tagA = (u32)T, tagB = (u32)(T >> 32);
    Tg g0{512u - t0, t0}, g1{512u - t1, t1}, g2{512u - t2, t2}, g3{512u - t3, t3};
    asm volatile("" : "+s"(g0.c), "+s"(g1.c), "+s"(g2.c), "+s"(g3.c), "+v"(g0.t), "+v"(g1.t), "+v"(g2.t), "+v"(g3.t));
    float wsum_r = 0.f;

    LAS f2* const Zw = (LAS f2*)(smem + L_Z) + w4 * 624;
    if (!hiw) {
        {
            float s = 0.f;
#pragma unroll
            for (int i = 0; i < 6; ++i) s += wr2[i].x + wr2[i].y;
            wsum_r = wsum(lane < 48 ? s : 0.f);
        }
        STAMP(1);
        const f2 c2 = f2{rdl(xv[0].x, 0), rdl(xv[0].y, 0)};
        f2 v[12];
#pragma unroll
        for (int i = 0; i < 12; ++i) v[i] = xv[i] - c2;
        f2 s1, s2;
        {
            f2 a[3], q[3];
#pragma unroll
            for (int i = 0; i < 3; ++i) { a[i] = v[i] + v[i + 3]; q[i] = v[i] * v[i]; }
#pragma unroll
            for (int i = 3; i < 12; ++i) { if (i >= 6) a[i % 3] += v[i]; q[i % 3] = pfma(v[i], v[i], q[i % 3]); }
            s1 = a[0] + a[1] + a[2]; s2 = q[0] + q[1] + q[2];
        }
        if (lane >= 48) { s1 = f2{0.f, 0.f}; s2 = f2{0.f, 0.f}; }
        s1 = f2{wsum(s1.x), wsum(s1.y)}; s2 = f2{wsum(s2.x), wsum(s2.y)};
        const f2 md = s1 * (1.f / 576.f);
        f2 var = pfma(-md, md, s2 * (1.f / 576.f));
        var = __builtin_elementwise_max(var, f2{0.f, 0.f});
        const f2 m = rbf2(c2 + md);
        const f2 ve = rbf2(rbf2(var) + 1e-5f);
        const f2 sd = rbf2(f2{__builtin_amdgcn_sqrtf(ve.x), __builtin_amdgcn_sqrtf(ve.y)});
        const f2 rsd = f2{__builtin_amdgcn_rcpf(sd.x), __builtin_amdgcn_rcpf(sd.y)};
        const f2 off = (c2 - m) * rsd;
        if (lane < 48) {
#pragma unroll
            for (int i = 0; i < 12; ++i) v[i] = sumdiff(pfma(v[i], rsd, off));
            r12<1>(v);
#pragma unroll
            for (int i = 1; i < 12; ++i) v[i] = cmul(v[i], tw1[i]);
#pragma unroll
            for (int i = 0; i < 12; ++i) Zw[i * 52 + lane] = v[i];
            CFENCE();
#pragma unroll
            for (int i = 0; i < 12; ++i) v[i] = Zw[(lane >> 2) * 52 + 4 * i + (lane & 3)];
            r12<1>(v);
#pragma unroll
            for (int i = 1; i < 12; ++i) v[i] = cmul(v[i], tw2[i]);
            CFENCE();
#pragma unroll
            for (int i = 0; i < 12; ++i) Zw[(lane & 3) * 148 + (lane >> 2) + 12 * i] = v[i];
            CFENCE();
#pragma unroll
            for (int g = 0; g < 3; ++g)
#pragma unroll
                for (int c = 0; c < 4; ++c) v[g * 4 + c] = Zw[c * 148 + lane + 48 * g];
#pragma unroll
            for (int g = 0; g < 3; ++g) r4<1>(v[g * 4], v[g * 4 + 1], v[g * 4 + 2], v[g * 4 + 3]);
            {
                f2 P = add_pi(v[0], v[2]), Q = add_mi(v[0], v[2]);
                if (lane == 0) { v[0] = P; v[2] = Q; }
            }
            CFENCE();
            Zw[lane == 0 ? 577 : lane + 289] = v[2];
#pragma unroll
            for (int g = 0; g < 3; ++g) {
                Zw[lane + 48 * g] = v[g * 4]; Zw[lane + 48 * g + 144] = v[g * 4 + 1];
                if (g) Zw[lane + 48 * g + 289] = v[g * 4 + 2];
                Zw[lane + 48 * g + 433] = v[g * 4 + 3];
            }
        }
        STAMP(2);
    }
    bar();
    if (lane < 36) {
        const int h = w & 1, kap = (w >> 1) * 36 + lane;
        f2 u0[4], u1[4];
#pragma unroll
        for (int a = 0; a < 4; ++a) {
            const LAS f2* Za = (const LAS f2*)(smem + L_Z) + a * 624;
            f4 zp = *(const LAS f4*)(Za + 2 * kap), zm = *(const LAS f4*)(Za + 576 - 2 * kap);
            if (h == 0) { u0[a] = add_conj(zp.xy, zm.zw); u1[a] = add_conj(zp.zw, zm.xy); }
            else { u0[a] = odd_sep(zp.xy, zm.zw); u1[a] = odd_sep(zp.zw, zm.xy); }
        }
        if (h == 0) { r4<1>(u0[0], u0[1], u0[2], u0[3]); r4<1>(u1[0], u1[1], u1[2], u1[3]); }
        else { r4odd<1>(u0[0], u0[1], u0[2], u0[3]); r4odd<1>(u1[0], u1[1], u1[2], u1[3]); }
        if (w < 2) {
            if (h == 0) {
                f2 a1 = add_conj(u0[1], u0[3]), a3 = odd_sep(u0[3], u0[1]);
                if (lane == 0) { u0[1] = a1; u0[3] = a3; }
            } else {
                f2 a0 = add_conj(u0[0], u0[3]), a3 = odd_sep(u0[3], u0[0]), a1 = add_conj(u0[1], u0[2]), a2 = odd_sep(u0[2], u0[1]);
                if (lane == 0) { u0[0] = a0; u0[1] = a1; u0[2] = a2; u0[3] = a3; }
            }
        }
        const u32 o = WS_E1 + (((h * 36 + (kap >> 2)) * 256 + b) * 64 + (kap & 3) * 16);
#pragma unroll
        for (int rp = 0; rp < 4; ++rp) {
            const Tw cw = ctw(2 * rp + h);
            const f2 a = cmul(u0[rp], cw), c = cmul(u1[rp], cw);
            u32x4 g = {tagd(a.x, g0), tagd(a.y, g1), tagd(c.x, g2), tagd(c.y, g3)};
            __builtin_amdgcn_raw_buffer_store_b128(g, rs, o + rp * (72 * 256 * 64), 0, 16);
        }
    }
    STAMP(3);
    LAS f2* const TT = (LAS f2*)(smem + (hiw ? L_TB : L_TA));
    const int mp = lane >> 4, mt = (lane >> 1) & 7, me = lane & 1;
    const int jj = (lane >> 2) + 16 * w4, kp = lane & 3;
    TwA<8> H; f2 Hb[8];
    TwA<4> tw256; TwA<8> tw64;
    const bool s0w = (b < 8) && (w == 0);
    if (p2act) {
        const LAS u32* wst = (const LAS u32*)(smem + (hiw ? L_WSTB : L_WST)) + w4 * 512 + (4 * mt + mp) * 2 + me;
        const float sc = 0.5f / (2048.f * 576.f);
#pragma unroll
        for (int d = 0; d < 8; ++d) { const f2 hv = unbf(wst[d * 64]) * sc; H.set(d, hv.x, f2{-hv.y, hv.y}); }
        if (s0w) {
#pragma unroll
            for (int d = 0; d < 8; ++d) {
                f4 s = *(const LAS f4*)(smem + L_S0 + (32 * d + 4 * mt + mp) * 16);
                if (me == 0) H.set(d, s.x, f2{-s.y, s.y});
                Hb[d] = me == 0 ? s.zw : f2{0.f, 0.f};
            }
        }
        H.pin(); asm volatile("" : "+v"(H.n[0]));
        twfill(tw256, jj, 1.f / 256.f); twfill(tw64, mt, 1.f / 64.f);
        vm0();
        u32x4 g[4];
        const u32 o = WS_E1 + ((cls * 36 + kt) * 256 + jj) * 64 + kp * 16;
        u32 pend = 15u;
        for (int spin = 0; spin < SPIN_CAP; ++spin) {
#pragma unroll
            for (int i = 0; i < 4; ++i) if (pend >> i & 1) g[i] = __builtin_amdgcn_raw_buffer_load_b128(rs, o, i * 4096, 16);
#pragma unroll
            for (int i = 0; i < 4; ++i) if (pend >> i & 1) {
                u32 bad = ((g[i].x ^ t0) | (g[i].y ^ t1) | (g[i].z ^ t2) | (g[i].w ^ t3)) & 1023u;
                if (__builtin_amdgcn_ballot_w64(bad != 0) == 0) pend &= ~(1u << i);
            }
            if (!pend) break;
            __builtin_amdgcn_s_sleep(1); CFENCE();
        }
        STAMP(4);
        f2 a[4], c[4];
#pragma unroll
        for (int i = 0; i < 4; ++i) { a[i] = f2{__uint_as_float(g[i].x), __uint_as_float(g[i].y)}; c[i] = f2{__uint_as_float(g[i].z), __uint_as_float(g[i].w)}; }
        r4<1>(a[0], a[1], a[2], a[3]); r4<1>(c[0], c[1], c[2], c[3]);
#pragma unroll
        for (int p = 0; p < 4; ++p) {
            if (p) { a[p] = cmul(a[p], tw256[p]); c[p] = cmul(c[p], tw256[p]); }
            *(LAS f4*)(TT + kp * 612 + p * 144 + jj * 2) = f4{a[p].x, a[p].y, c[p].x, c[p].y};
        }
    }
    bar();
    if (hiw && moe) {
        const int s = ((b - 80) & 7) * 18 + ((b - 80) >> 3);
        const u16* gsrc = wg + (size_t)(lane >> 2) * 4608 + s * 32 + (lane & 3) * 8;
        const long du = wu - wg;
        const u16* dsrc = wd + (size_t)s * 32 * 576 + lane * 8;
#pragma unroll 1
        for (int pc = N1_DMA + w4; pc < 108; pc += 4) {
            const u16* src = pc < 36 ? gsrc + (size_t)pc * 16 * 4608 : pc < 72 ? gsrc + du + (size_t)(pc - 36) * 16 * 4608 : dsrc + (pc - 72) * 512;
            glds16(src, lbase + L_BIG + pc * 1024);
        }
    }
    if (p2act) {
        f2 v[8];
        const LAS f2* rd = TT + w4 * 612 + mp * 144 + mt * 2 + me;
#pragma unroll
        for (int i = 0; i < 8; ++i) v[i] = rd[16 * i];
        r8<1>(v);
#pragma unroll
        for (int c = 1; c < 8; ++c) v[c] = cmul(v[c], tw64[c]);
        LAS f2* bx = TT + w4 * 608 + mp * 130 + me * 8;
        CFENCE();
#pragma unroll
        for (int c = 0; c < 8; ++c) bx[c * 16 + mt] = v[c];
        CFENCE();
#pragma unroll
        for (int t = 0; t < 8; t += 2) { f4 z = *(const LAS f4*)(bx + mt * 16 + t); v[t] = z.xy; v[t + 1] = z.zw; }
        r8<1>(v);
        if (s0w && (cls & 3) == 0) {
            LAS f2* ex = (LAS f2*)(smem + L_S0 + 4096);
            const int q0 = 4 * mt + mp;
            if (me == 0) {
#pragma unroll
                for (int d = 0; d < 8; ++d) ex[32 * d + q0] = v[d];
            }
            CFENCE();
#pragma unroll
            for (int d = 0; d < 8; ++d) {
                const int q = 32 * d + q0;
                f2 xm = ex[cls == 0 ? (256 - q) & 255 : 255 - q];
                f2 t = cmul(v[d], H[d]);
                f2 u = cmulc(Hb[d], mktw(xm));
                v[d] = t + u;
            }
        } else {
#pragma unroll
            for (int d = 0; d < 8; ++d) v[d] = cmul(v[d], H[d]);
        }
        r8<-1>(v);
#pragma unroll
        for (int c = 1; c < 8; ++c) v[c] = cmulc(v[c], tw64[c]);
        CFENCE();
#pragma unroll
        for (int c = 0; c < 8; ++c) bx[c * 16 + mt] = v[c];
        CFENCE();
#pragma unroll
        for (int t = 0; t < 8; t += 2) { f4 z = *(const LAS f4*)(bx + mt * 16 + t); v[t] = z.xy; v[t + 1] = z.zw; }
        r8<-1>(v);
        CFENCE();
        LAS f2* wrp = TT + w4 * 616 + mp * 144 + mt * 2 + me;
#pragma unroll
        for (int i = 0; i < 8; ++i) wrp[16 * i] = v[i];
    }
    bar();
    if (p2act) {
        f2 a[4], c[4];
#pragma unroll
        for (int p = 0; p < 4; ++p) {
            f4 z = *(const LAS f4*)(TT + kp * 616 + p * 144 + jj * 2);
            a[p] = z.xy; c[p] = z.zw;
            if (p) { a[p] = cmulc(a[p], tw256[p]); c[p] = cmulc(c[p], tw256[p]); }
        }
        r4<-1>(a[0], a[1], a[2], a[3]); r4<-1>(c[0], c[1], c[2], c[3]);
        const u32 o = WS_E2 + jj * 18432 + (cls * 36 + kt) * 64 + kp * 16;
#pragma unroll
        for (int i = 0; i < 4; ++i) {
            u32x4 g = {tagd(a[i].x, g0), tagd(a[i].y, g1), tagd(c[i].x, g2), tagd(c[i].y, g3)};
            __builtin_amdgcn_raw_buffer_store_b128(g, rs, o + i * 64 * 18432, 0, 16);
        }
    }
    STAMP(5);
    TwA<12> tv1, tv2;
    if (!hiw) { int lr = l48; asm volatile("" : "+v"(lr)); twfill(tv1, lr, 1.f / 576.f); twfill(tv2, lr & 3, 1.f / 48.f); }
    vm0();
    if (lane < 36) {
        const int k = w * 36 + lane;
        const u32 ta = (lane & 1) ? t2 : t0, tb = (lane & 1) ? t3 : t1;
        const u32 o = WS_E2 + b * 18432 + k * 8;
        u32x2 g[8];
        u32 pend = 255u;
        for (int spin = 0; spin < SPIN_CAP; ++spin) {
#pragma unroll
            for (int r = 0; r < 8; ++r) if (pend >> r & 1) g[r] = __builtin_amdgcn_raw_buffer_load_b64(rs, o + r * 2304, 0, 16);
#pragma unroll
            for (int r = 0; r < 8; ++r) if (pend >> r & 1) {
                u32 bad = ((g[r].x ^ ta) | (g[r].y ^ tb)) & 1023u;
                if (__builtin_amdgcn_ballot_w64(bad != 0) == 0) pend &= ~(1u << r);
            }
            if (!pend) break;
            __builtin_amdgcn_s_sleep(1); CFENCE();
        }
        STAMP(6);
        f2 v[8];
#pragma unroll
        for (int r = 0; r < 8; ++r) { v[r] = f2{__uint_as_float(g[r].x), __uint_as_float(g[r].y)}; if (r) v[r] = cmulc(v[r], ctw(r)); }
        if (w == 0) {
            f2 c2 = v[2] + v[6].yx, c6 = (v[2] - v[6].yx) * PM;
            f2 c1 = v[1] + v[7].yx, c7 = (v[1] - v[7].yx) * PM;
            f2 c3 = v[3] + v[5].yx, c5 = (v[3] - v[5].yx) * PM;
            if (lane == 0) { v[2] = c2; v[6] = c6; v[1] = c1; v[7] = c7; v[3] = c3; v[5] = c5; }
        }
        r4<-1>(v[0], v[2], v[4], v[6]);
        r4<-1>(v[1], v[3], v[5], v[7]);
        LAS f2* Z0 = (LAS f2*)(smem + L_Z);
        const f2 hh = f2{C_H, C_H};
        Z0[k] = add_pi(v[0], v[1]); Z0[577 - k] = add_mi(v[0], v[1]);
        { f2 t = nadd_pi(v[3], v[3]); Z0[624 + k] = pfma(t, hh, v[2]); Z0[624 + 577 - k] = pfma(t, -hh, v[2]); }
        Z0[2 * 624 + k] = v[4] - v[5]; Z0[2 * 624 + 577 - k] = v[4] + v[5];
        { f2 t = nadd_mi(v[7], v[7]); Z0[3 * 624 + k] = pfma(t, hh, v[6]); Z0[3 * 624 + 577 - k] = pfma(t, -hh, v[6]); }
    }
    bar();
    u32 po[12], ph[12], pl[12];
    s16x4 fa[18], fd[10];
    const int tq = (lane & 15) >> 2, tp = lane & 3, tg = lane >> 4;
    auto load_frags = [&]() {
        const u32 mb = lbase + ((w & 2) ? L_WU : L_WG) + (w & 1) * 32 + tp * 8;
#pragma unroll
        for (int s = 0; s < 9; ++s)
#pragma unroll
            for (int t = 0; t < 2; ++t) {
                const int p = 288 * (w >> 2) + 32 * s + 8 * tg + 4 * t + tq;
                const int row = p / 12 + 48 * (p % 12);
                fa[s * 2 + t] = __builtin_bit_cast(s16x4, __builtin_amdgcn_ds_read_tr16_b64_v4i16((LAS s16x4*)(uintptr_t)(mb + row * 64)));
            }
#pragma unroll
        for (int i = 0; i < 5; ++i) {
            const int ct = (i < 4) ? w + 8 * i : 32 + w4;
#pragma unroll
            for (int t = 0; t < 2; ++t)
                fd[i * 2 + t] = __builtin_bit_cast(s16x4, __builtin_amdgcn_ds_read_tr16_b64_v4i16((LAS s16x4*)(uintptr_t)(lbase + L_WD + (8 * tg + 4 * t + tq) * 1152 + ct * 32 + tp * 8)));
        }
    };
    if (!hiw) {
        f2 v[12];
        if (lane < 48) {
            v[2] = Zw[lane == 0 ? 577 : lane + 289];
#pragma unroll
            for (int g = 0; g < 3; ++g) {
                v[g * 4] = Zw[lane + 48 * g]; v[g * 4 + 1] = Zw[lane + 48 * g + 144];
                if (g) v[g * 4 + 2] = Zw[lane + 48 * g + 289];
                v[g * 4 + 3] = Zw[lane + 48 * g + 433];
            }
            {
                f2 z0 = add_conj(v[0], v[2]), d = sub_conj(v[0], v[2]);
                if (lane == 0) { v[0] = z0; v[2] = d.yx; }
            }
#pragma unroll
            for (int g = 0; g < 3; ++g) {
                f2 s0 = add_conj(v[g * 4], v[g * 4 + 2]), s1 = sub_conj(v[g * 4], v[g * 4 + 2]);
                f2 s2 = add_conj(v[g * 4 + 1], v[g * 4 + 3]), s3 = sub_conj(v[g * 4 + 1], v[g * 4 + 3]);
                v[g * 4] = s0 + s2; v[g * 4 + 2] = s0 - s2; v[g * 4 + 1] = add_pi(s1, s3); v[g * 4 + 3] = add_mi(s1, s3);
            }
            CFENCE();
#pragma unroll
            for (int g = 0; g < 3; ++g)
#pragma unroll
                for (int c = 0; c < 4; ++c) Zw[c * 152 + lane + 48 * g] = v[g * 4 + c];
            CFENCE();
#pragma unroll
            for (int i = 0; i < 12; ++i) v[i] = Zw[(lane & 3) * 152 + (lane >> 2) + 12 * i];
#pragma unroll
            for (int i = 1; i < 12; ++i) v[i] = cmulc(v[i], tv2[i]);
            r12<-1>(v);
            CFENCE();
#pragma unroll
            for (int i = 0; i < 12; ++i) Zw[(lane >> 2) * 52 + 4 * i + (lane & 3)] = v[i];
            CFENCE();
#pragma unroll
            for (int i = 0; i < 12; ++i) v[i] = Zw[i * 52 + lane];
#pragma unroll
            for (int i = 1; i < 12; ++i) v[i] = cmulc(v[i], tv1[i]);
            r12<-1>(v);
        }
        STAMP(7);
#pragma unroll
        for (int i = 0; i < 12; ++i) { ph[i] = pkbf(xv[i] + rbf2(sumdiff(v[i]))); v[i] = unbf(ph[i]); }
        const f2 c2 = f2{rdl(v[0].x, 0), rdl(v[0].y, 0)};
        f2 s1, s2, s3;
        {
            f2 d[12], a[2], q[2], c[2];
#pragma unroll
            for (int i = 0; i < 12; ++i) d[i] = v[i] - c2;
#pragma unroll
            for (int i = 0; i < 2; ++i) { a[i] = d[i] + d[i + 2]; q[i] = d[i] * d[i]; c[i] = d[i] * (i ? wr2[0].yy : wr2[0].xx); }
#pragma unroll
            for (int i = 2; i < 12; ++i) { if (i >= 4) a[i & 1] += d[i]; q[i & 1] = pfma(d[i], d[i], q[i & 1]); c[i & 1] = pfma(d[i], (i & 1) ? wr2[i / 2].yy : wr2[i / 2].xx, c[i & 1]); }
            s1 = a[0] + a[1]; s2 = q[0] + q[1]; s3 = c[0] + c[1];
        }
        if (lane >= 48) { s1 = f2{0.f, 0.f}; s2 = f2{0.f, 0.f}; s3 = f2{0.f, 0.f}; }
        s1 = f2{wsum(s1.x), wsum(s1.y)}; s2 = f2{wsum(s2.x), wsum(s2.y)}; s3 = f2{wsum(s3.x), wsum(s3.y)};
        const f2 md = s1 * (1.f / 576.f);
        f2 var = pfma(-md, md, s2 * (1.f / 576.f));
        var = __builtin_elementwise_max(var, f2{0.f, 0.f});
        const f2 m = rbf2(c2 + md);
        const f2 ve = rbf2(rbf2(var) + 1e-5f);
        const f2 sd = rbf2(f2{__builtin_amdgcn_sqrtf(ve.x), __builtin_amdgcn_sqrtf(ve.y)});
        const f2 rsd = f2{__builtin_amdgcn_rcpf(sd.x), __builtin_amdgcn_rcpf(sd.y)};
        const u32 rb = pkbf(pfma(c2 - m, f2{wsum_r, wsum_r}, s3) * rsd);
        if (lane < 2) {
            u32 bits = lane ? rb >> 16 : rb & 0xFFFFu;
            u32 key = (bits & 0x8000u) ? (bits ^ 0xFFFFu) : (bits | 0x8000u);
            if ((bits & 0x7FFFu) > 0x7F80u) key = 0;
            const u32 row = b + 256 * (w4 + 4 * lane);
            ((LAS u32*)(smem + L_KEYS))[w4 + 4 * lane] = (key << 16) | (0xFFFFu - row);
        }
#pragma unroll
        for (int i = 0; i < 12; ++i) {
            pl[i] = pkbf(rbf2(v[i] - m) * rsd);
            po[i] = pkbf(v[i] + unbf(pl[i]));
        }
    } else if (moe) {
        load_frags();
    }
    bar();
    STAMP(8);
    u32 bk1, bk2;
    {
        const u32x4 ka = *(const LAS u32x4*)(smem + L_KEYS), kb = *(const LAS u32x4*)(smem + L_KEYS + 16);
        u32 k[8] = {ka.x, ka.y, ka.z, ka.w, kb.x, kb.y, kb.z, kb.w};
        bk1 = 0; bk2 = 0;
#pragma unroll
        for (int i = 0; i < 8; ++i) { u32 lo = k[i] < bk1 ? k[i] : bk1; bk1 = k[i] > bk1 ? k[i] : bk1; bk2 = lo > bk2 ? lo : bk2; }
        bk1 = __builtin_amdgcn_readfirstlane(bk1); bk2 = __builtin_amdgcn_readfirstlane(bk2);
    }
    auto publish_row = [&](const u32* pk, int hi, u32 off) {
        if (lane < 48) {
            u32 q[6];
#pragma unroll
            for (int i = 0; i < 6; ++i) q[i] = hi ? ((pk[2 * i] >> 16) | (pk[2 * i + 1] & 0xFFFF0000u)) : ((pk[2 * i] & 0xFFFFu) | (pk[2 * i + 1] << 16));
            __builtin_amdgcn_raw_buffer_store_b128(u32x4{q[0], q[1], q[2], tagA}, rs, off + lane * 32, 0, 16);
            __builtin_amdgcn_raw_buffer_store_b128(u32x4{q[3], q[4], q[5], tagA}, rs, off + lane * 32 + 16, 0, 16);
        }
    };
    if (!hiw) {
#pragma unroll
        for (int hi = 0; hi < 2; ++hi) {
            const u32 row = b + 256 * (w4 + 4 * hi);
            if (((0xFFFFu - row) == (bk1 & 0xFFFFu)) || ((0xFFFFu - row) == (bk2 & 0xFFFFu))) publish_row(pl, hi, WS_ROWL + row * 1536);
        }
        if (moe) load_frags();
    } else if (w == 4) {
        if (lane == 0) __builtin_amdgcn_raw_buffer_store_b128(u32x4{bk1, bk2, tagA, tagB}, rs, WS_CAND + b * 16, 0, 16);
        u32 m1 = 0, m2 = 0;
        for (int spin = 0; spin < SPIN_CAP; ++spin) {
            u32x4 g[4];
#pragma unroll
            for (int i = 0; i < 4; ++i) g[i] = __builtin_amdgcn_raw_buffer_load_b128(rs, WS_CAND + lane * 16, i * 1024, 16);
            u32 bad = 0;
#pragma unroll
            for (int i = 0; i < 4; ++i) bad |= (g[i].z ^ tagA) | (g[i].w ^ tagB);
            m1 = 0; m2 = 0;
#pragma unroll
            for (int i = 0; i < 4; ++i) {
                u32 lo = g[i].x < m1 ? g[i].x : m1; m1 = g[i].x > m1 ? g[i].x : m1;
                lo = g[i].y > lo ? g[i].y : lo; m2 = lo > m2 ? lo : m2;
            }
            if (__builtin_amdgcn_ballot_w64(bad != 0) == 0) break;
            __builtin_amdgcn_s_sleep(1); CFENCE();
        }
        const u32 K1 = wmax(m1);
        const u32 K2 = wmax(m1 == K1 ? m2 : m1);
        STAMP(9);
        if (lane == 0) *(LAS u32x2*)(smem + L_TOP) = u32x2{K1, K2};
        if (moe) {
            const u32 r1 = (0xFFFFu - (K1 & 0xFFFFu)) & 2047u, r2 = (0xFFFFu - (K2 & 0xFFFFu)) & 2047u;
            u32x4 g[3];
            u32 of[3], ld[3];
#pragma unroll
            for (int i = 0; i < 3; ++i) { const u32 n = lane + 64 * i; const bool s = n >= 96; of[i] = WS_ROWL + (s ? r2 : r1) * 1536 + (s ? n - 96 : n) * 16; ld[i] = L_XS + n * 12; }
            for (int spin = 0; spin < SPIN_CAP; ++spin) {
                u32 bad = 0;
#pragma unroll
                for (int i = 0; i < 3; ++i) g[i] = __builtin_amdgcn_raw_buffer_load_b128(rs, of[i], 0, 16);
#pragma unroll
                for (int i = 0; i < 3; ++i) bad |= g[i].w ^ tagA;
                if (__builtin_amdgcn_ballot_w64(bad != 0) == 0) break;
                __builtin_amdgcn_s_sleep(1); CFENCE();
            }
#pragma unroll
            for (int i = 0; i < 3; ++i) { LAS u32* d = (LAS u32*)(smem + ld[i]); d[0] = g[i].x; d[1] = g[i].y; d[2] = g[i].z; }
        }
        STAMP(10);
    }
    bar();
    if (b == 0 && threadIdx.x == 448) __hip_atomic_store((u64*)(ws + WS_EPOCH), epoch + 1, __ATOMIC_RELAXED, __HIP_MEMORY_SCOPE_AGENT);
    const u32x2 top = *(const LAS u32x2*)(smem + L_TOP);
    const u32 K1 = __builtin_amdgcn_readfirstlane(top.x), K2 = __builtin_amdgcn_readfirstlane(top.y);
    const u32 row1 = (0xFFFFu - (K1 & 0xFFFFu)) & 2047u, row2 = (0xFFFFu - (K2 & 0xFFFFu)) & 2047u;
    if (!hiw) {
#pragma unroll
        for (int hi = 0; hi < 2; ++hi) {
            const u32 row = b + 256 * (w4 + 4 * hi);
            if (row == row1) publish_row(ph, hi, WS_ROWH);
            if (row == row2) publish_row(ph, hi, WS_ROWH + 1536);
        }
    }
    if (moe) {
        const int s = ((b - 80) & 7) * 18 + ((b - 80) >> 3);
        f4 acc = {0.f, 0.f, 0.f, 0.f};
        const LAS char* xs = (const LAS char*)(smem + L_XS) + (lane & 1) * 1152 + (w >> 2) * 576 + tg * 16;
#pragma unroll
        for (int st = 0; st < 9; ++st) {
            const bf8 bfr = *(const LAS bf8*)(xs + st * 64);
            const s16x8 af = __builtin_shufflevector(fa[2 * st], fa[2 * st + 1], 0, 1, 2, 3, 4, 5, 6, 7);
            acc = __builtin_amdgcn_mfma_f32_16x16x32_bf16(__builtin_bit_cast(bf8, af), bfr, acc, 0, 0, 0);
        }
        if ((lane & 15) < 2) *(LAS f4*)(smem + L_GU + w * 128 + (lane & 1) * 64 + tg * 16) = acc;
        bar();
        {
            const int tok = lane >> 5, f = lane & 31;
            const LAS float* gu = (const LAS float*)(smem + L_GU) + (f >> 4) * 32 + tok * 16 + (f & 15);
            const float g = rbf(gu[0] + gu[4 * 32]), u = rbf(gu[2 * 32] + gu[6 * 32]);
            const float sg = rbf(g * __builtin_amdgcn_rcpf(1.f + __builtin_amdgcn_exp2f(-1.4426950408889634f * g)));
            const u32 ab = pkbf(f2{sg * u, 0.f});
            ((LAS u16*)(smem + L_AB + w * 128))[lane] = (u16)ab;
        }
        CFENCE();
        const bf8 bfr = *(const LAS bf8*)(smem + L_AB + w * 128 + (lane & 1) * 64 + tg * 16);
#pragma unroll
        for (int i = 0; i < 5; ++i) {
            if (i == 4 && hiw) break;
            const int ct = (i < 4) ? w + 8 * i : 32 + w4;
            const s16x8 af = __builtin_shufflevector(fd[2 * i], fd[2 * i + 1], 0, 1, 2, 3, 4, 5, 6, 7);
            const f4 d = __builtin_amdgcn_mfma_f32_16x16x32_bf16(__builtin_bit_cast(bf8, af), bfr, f4{0.f, 0.f, 0.f, 0.f}, 0, 0, 0);
            if ((lane & 15) < 2) {
                u32x4 g = {tagd(d.x, g0), tagd(d.y, g1), tagd(d.z, g2), tagd(d.w, g3)};
                __builtin_amdgcn_raw_buffer_store_b128(g, rs, WS_PB + (((lane & 1) * 36 + ct) * 144 + s) * 64 + tg * 16, 0, 16);
            }
        }
        STAMP(11);
    }
    if (!hiw) {
        if (lane < 48) {
#pragma unroll
            for (int hi = 0; hi < 2; ++hi) {
                const u32 row = b + 256 * (w4 + 4 * hi);
                if (row != row1 && row != row2) {
                    u16* o = out + (size_t)row * 576 + lane;
#pragma unroll
                    for (int i = 0; i < 12; ++i) o[48 * i] = (u16)(hi ? po[i] >> 16 : po[i]);
                }
            }
        }
    } else if (fin && w == 5) {
        const int ch = b - 8, tok = ch / 36, ct = ch % 36;
        const u32 row = tok ? row2 : row1, K = tok ? K2 : K1;
        const int c0 = ct * 16, ii = c0 / 48;
        const u32 go = (2 * (c0 % 48 + (lane & 15)) + (ii >= 6 ? 1 : 0)) * 16;
        const int e = ii % 6;
        u32x4 gl, gh;
        for (int spin = 0; spin < SPIN_CAP; ++spin) {
            gl = __builtin_amdgcn_raw_buffer_load_b128(rs, WS_ROWL + row * 1536 + go, 0, 16);
            gh = __builtin_amdgcn_raw_buffer_load_b128(rs, WS_ROWH + tok * 1536 + go, 0, 16);
            if (__builtin_amdgcn_ballot_w64(((gl.w ^ tagA) | (gh.w ^ tagA)) != 0) == 0) break;
            __builtin_amdgcn_s_sleep(2); CFENCE();
        }
        const u32 dl = e < 2 ? gl.x : e < 4 ? gl.y : gl.z, dh = e < 2 ? gh.x : e < 4 ? gh.y : gh.z;
        const float lv = (e & 1) ? bfh(dl) : bfl(dl), hv = (e & 1) ? bfh(dh) : bfl(dh);
        const u32 kb = K >> 16;
        const float rv = bfl((kb & 0x8000u) ? (kb & 0x7FFFu) : (kb ^ 0xFFFFu));
        const float rsel = rbf(__builtin_amdgcn_rcpf(1.f + __builtin_amdgcn_exp2f(-1.4426950408889634f * rv)));
        u32x4 g[9];
        const u32 o = WS_PB + ch * 9216 + lane * 16;
        u32 pend = 511u;
        for (int spin = 0; spin < SPIN_CAP; ++spin) {
#pragma unroll
            for (int i = 0; i < 9; ++i) if (pend >> i & 1) g[i] = __builtin_amdgcn_raw_buffer_load_b128(rs, o, i * 1024, 16);
#pragma unroll
            for (int i = 0; i < 9; ++i) if (pend >> i & 1) {
                u32 bad = ((g[i].x ^ t0) | (g[i].y ^ t1) | (g[i].z ^ t2) | (g[i].w ^ t3)) & 1023u;
                if (__builtin_amdgcn_ballot_w64(bad != 0) == 0) pend &= ~(1u << i);
            }
            if (!pend) break;
            __builtin_amdgcn_s_sleep(2); CFENCE();
        }
        STAMP(12);
        f4 a = {0.f, 0.f, 0.f, 0.f};
#pragma unroll
        for (int i = 0; i < 9; ++i) a += __builtin_bit_cast(f4, g[i]);
        float r[4] = {a.x, a.y, a.z, a.w};
#pragma unroll
        for (int i = 0; i < 4; ++i) {
            r[i] = dppa<0x124, 0xF>(r[i]);
            r[i] = dppa<0x128, 0xF>(r[i]);
            r[i] += __shfl_xor(r[i], 16); r[i] += __shfl_xor(r[i], 32);
        }
        const int src = (lane & 15) >> 2;
        float y = 0.f;
#pragma unroll
        for (int i = 0; i < 4; ++i) { float t = __shfl(r[i], src); if ((lane & 3) == i) y = t; }
        const float upd = rbf(rbf(y) * rsel);
        const u32 ob = pkbf(f2{hv + rbf(lv + upd), 0.f});
        if (lane < 16) out[(size_t)row * 576 + c0 + lane] = (u16)ob;
        STAMP(13);
    }
}

extern "C" void kernel_launch(void* const* d_in, const int* in_sizes, int n_in, void* d_out, int out_size, void* d_ws, size_t ws_size,
                              hipStream_t stream) {
    (void)in_sizes; (void)n_in; (void)out_size; (void)ws_size;
    (void)hipFuncSetAttribute((const void*)fused_kernel, hipFuncAttributeMaxDynamicSharedMemorySize, L_TOTAL);
    fused_kernel<<<256, 512, L_TOTAL, stream>>>((const u16*)d_in[0], (const u16*)d_in[1], (const u16*)d_in[2], (const u16*)d_in[4],
                                               (const u16*)d_in[5], (const u16*)d_in[6], (const u16*)d_in[7], (u16*)d_out, (char*)d_ws);
}
```

```cpp
#define PAD_US 40
#include <hip/hip_runtime.h>
#include <stdint.h>

typedef unsigned short u16;
typedef unsigned int u32;
typedef unsigned long long u64;
typedef float f2 __attribute__((ext_vector_type(2)));
typedef float f4 __attribute__((ext_vector_type(4)));
typedef u32 u32x2 __attribute__((ext_vector_type(2)));
typedef u32 u32x4 __attribute__((ext_vector_type(4)));
typedef __bf16 bf2 __attribute__((ext_vector_type(2)));
typedef __bf16 bf8 __attribute__((ext_vector_type(8)));
typedef short s16x4 __attribute__((ext_vector_type(4)));
typedef short s16x8 __attribute__((ext_vector_type(8)));
#define DEV __device__ __forceinline__
#define LAS __attribute__((address_space(3)))

#define N1_DMA 56
#define SPIN_CAP 400000

#define WS_EPOCH   0u
#define WS_CAND    4096u
#define WS_ROWH    8192u
#define WS_PB      16384u
#define WS_ROWL    (1u << 20)
#define WS_E1      (5u << 20)
#define WS_E2      (10u << 20)
#define WS_BYTES   (15u << 20)
#define WS_STAMP   (16u << 20)
#define L_Z        0u
#define L_TA       19968u
#define L_WST      39488u
#define L_SP       47680u
#define L_KEYS     (L_SP)
#define L_TOP      (L_SP + 64u)
#define L_XS       (L_SP + 128u)
#define L_GU       (L_SP + 2560u)
#define L_AB       (L_SP + 3584u)
#define L_BIG      52288u
#define L_WG       (L_BIG)
#define L_WU       (L_BIG + 36864u)
#define L_WD       (L_BIG + 73728u)
#define L_TB       (L_BIG)
#define L_WSTB     (L_BIG + 19520u)
#define L_S0       (L_BIG + 32768u)
#define L_TOTAL    (L_BIG + 110592u)

DEV f2 pfma(f2 a, f2 b, f2 c) { return __builtin_elementwise_fma(a, b, c); }
#define PM f2{1.f, -1.f}
#define MP f2{-1.f, 1.f}
DEV f2 add_mi(f2 a, f2 b) { return pfma(b.yx, PM, a); }
DEV f2 add_pi(f2 a, f2 b) { return pfma(b.yx, MP, a); }
DEV f2 nadd_mi(f2 a, f2 b) { return pfma(b.yx, PM, -a); }
DEV f2 nadd_pi(f2 a, f2 b) { return pfma(b.yx, MP, -a); }
DEV f2 add_conj(f2 a, f2 b) { return pfma(b, PM, a); }
DEV f2 sub_conj(f2 a, f2 b) { return pfma(b, MP, a); }
DEV f2 odd_sep(f2 a, f2 b) { return pfma(a.yx, PM, b.yx); }
DEV f2 sumdiff(f2 a) { return pfma(a.yy, PM, a.xx); }
struct Tw { f2 x; f2 n; };
DEV Tw mktw(f2 w) { return Tw{w.xx, f2{-w.y, w.y}}; }
DEV f2 cmul(f2 a, Tw w) { return pfma(a.yx, w.n, a * w.x); }
DEV f2 cmulc(f2 a, Tw w) { return pfma(a.yx, -w.n, a * w.x); }
template<int N> struct TwA {
    f2 xp[(N + 1) / 2]; f2 n[N];
    DEV Tw operator[](int i) const { return Tw{(i & 1) ? xp[i / 2].yy : xp[i / 2].xx, n[i]}; }
    DEV void set(int i, float x, f2 nn) { if (i & 1) xp[i / 2].y = x; else xp[i / 2].x = x; n[i] = nn; }
    DEV void pin() {
#pragma unroll
        for (int i = 0; i < (N + 1) / 2; ++i) asm volatile("" : "+v"(xp[i]));
#pragma unroll
        for (int i = 1; i < N; ++i) asm volatile("" : "+v"(n[i]));
    }
};
template<int DIR> DEV f2 addr_(f2 a, f2 b) { return DIR > 0 ? add_mi(a, b) : add_pi(a, b); }
template<int DIR> DEV f2 subr_(f2 a, f2 b) { return DIR > 0 ? add_pi(a, b) : add_mi(a, b); }
template<int DIR> DEV f2 naddr_(f2 a, f2 b) { return DIR > 0 ? nadd_mi(a, b) : nadd_pi(a, b); }

#define C_H 0.70710678118654752440f
#define C_K 0.86602540378443864676f
template<int DIR> DEV void r4(f2& a, f2& b, f2& c, f2& d) {
    f2 s0 = a + c, s1 = a - c, s2 = b + d, s3 = b - d;
    a = s0 + s2; c = s0 - s2; b = addr_<DIR>(s1, s3); d = subr_<DIR>(s1, s3);
}
template<int DIR> DEV void r3(f2& a, f2& b, f2& c) {
    f2 t = b + c, d = b - c;
    f2 m = pfma(t, f2{-0.5f, -0.5f}, a);
    a = a + t;
    const f2 kk = DIR > 0 ? f2{C_K, -C_K} : f2{-C_K, C_K};
    b = pfma(d.yx, kk, m);
    c = pfma(d.yx, -kk, m);
}
template<int DIR> DEV void r4odd(f2& a, f2& b, f2& c, f2& d) {
    f2 o0 = addr_<DIR>(a, c), o2 = subr_<DIR>(a, c);
    f2 p = addr_<DIR>(b, d), m = subr_<DIR>(b, d);
    f2 tp = addr_<DIR>(p, p), tm = naddr_<DIR>(m, m);
    const f2 hh = f2{C_H, C_H};
    a = pfma(tp, hh, o0); c = pfma(tp, -hh, o0); b = pfma(tm, hh, o2); d = pfma(tm, -hh, o2);
}
template<int DIR> DEV void r8(f2* v) {
    f2 a0 = v[0] + v[4], a4 = v[0] - v[4], a1 = v[1] + v[5], a5 = v[1] - v[5];
    f2 a2 = v[2] + v[6], a6 = v[2] - v[6], a3 = v[3] + v[7], a7 = v[3] - v[7];
    r4<DIR>(a0, a1, a2, a3);
    r4odd<DIR>(a4, a5, a6, a7);
    v[0] = a0; v[2] = a1; v[4] = a2; v[6] = a3; v[1] = a4; v[3] = a5; v[5] = a6; v[7] = a7;
}
template<int DIR> DEV void r12(f2* v) {
#pragma unroll
    for (int a = 0; a < 3; ++a) r4<DIR>(v[(4 * a) % 12], v[(4 * a + 3) % 12], v[(4 * a + 6) % 12], v[(4 * a + 9) % 12]);
#pragma unroll
    for (int b = 0; b < 4; ++b) r3<DIR>(v[(3 * b) % 12], v[(4 + 3 * b) % 12], v[(8 + 3 * b) % 12]);
    f2 o[12];
#pragma unroll
    for (int a = 0; a < 3; ++a)
#pragma unroll
        for (int b = 0; b < 4; ++b) o[(4 * a + 9 * b) % 12] = v[(4 * a + 3 * b) % 12];
#pragma unroll
    for (int i = 0; i < 12; ++i) v[i] = o[i];
}
template<int N> DEV void twfill(TwA<N>& t, int base, float invN) {
    const float st = (float)base * invN;
    t.xp[0].x = 1.f;
#pragma unroll
    for (int i = 1; i < N; ++i) { const float x = st * (float)i; const float sn = __builtin_amdgcn_sinf(x); t.set(i, __builtin_amdgcn_cosf(x), f2{sn, -sn}); }
    t.pin();
}

DEV float bfl(u32 u) { return __uint_as_float(u << 16); }
DEV float bfh(u32 u) { return __uint_as_float(u & 0xFFFF0000u); }
DEV u32 pkbf(f2 v) { bf2 b = __builtin_convertvector(v, bf2); return __builtin_bit_cast(u32, b); }
DEV f2 unbf(u32 u) { return f2{bfl(u), bfh(u)}; }
DEV f2 rbf2(f2 v) { return unbf(pkbf(v)); }
DEV float rbf(float v) { return bfl(pkbf(f2{v, v})); }

template<int CTRL, int RM> DEV float dppa(float v) { return v + __int_as_float(__builtin_amdgcn_update_dpp(0, __float_as_int(v), CTRL, RM, 0xF, false)); }
DEV float wsum(float v) {
    v = dppa<0x111, 0xF>(v); v = dppa<0x112, 0xF>(v); v = dppa<0x114, 0xF>(v); v = dppa<0x118, 0xF>(v);
    v = dppa<0x142, 0xA>(v); v = dppa<0x143, 0xC>(v);
    return __int_as_float(__builtin_amdgcn_readlane(__float_as_int(v), 63));
}
template<int CTRL, int RM> DEV u32 dppm(u32 v) { u32 o = (u32)__builtin_amdgcn_update_dpp(0, (int)v, CTRL, RM, 0xF, false); return v > o ? v : o; }
DEV u32 wmax(u32 v) {
    v = dppm<0x111, 0xF>(v); v = dppm<0x112, 0xF>(v); v = dppm<0x114, 0xF>(v); v = dppm<0x118, 0xF>(v);
    v = dppm<0x142, 0xA>(v); v = dppm<0x143, 0xC>(v);
    return (u32)__builtin_amdgcn_readlane((int)v, 63);
}
DEV float rdl(float v, int l) { return __int_as_float(__builtin_amdgcn_readlane(__float_as_int(v), l)); }

#define CFENCE() asm volatile("" ::: "memory")
DEV void bar() { asm volatile("s_waitcnt lgkmcnt(0)\n\ts_barrier" ::: "memory"); }
DEV void vm0() { asm volatile("s_waitcnt vmcnt(0)" ::: "memory"); }
DEV void glds16(const void* gsrc, u32 lds_dst) {
    u32 keep;
    asm volatile("s_mov_b32 %0, m0\n\ts_mov_b32 m0, %2\n\ts_nop 0\n\tglobal_load_lds_dwordx4 %1, off nt\n\ts_mov_b32 m0, %0" : "=&s"(keep) : "v"(gsrc), "s"(lds_dst) : "memory");
}
struct Tg { u32 c, t; };
DEV u32 tagd(float v, Tg g) { return ((__float_as_uint(v) + g.c) & 0xFFFFFC00u) | g.t; }

#define STAMP(i) do {} while (0)

__global__ void __launch_bounds__(512, 2) fused_kernel(const u16* __restrict__ x, const u16* __restrict__ awr, const u16* __restrict__ awi,
                                                       const u16* __restrict__ bwr, const u16* __restrict__ wg, const u16* __restrict__ wu,
                                                       const u16* __restrict__ wd, u16* out, char* ws) {
    extern __shared__ __attribute__((aligned(16))) char smem[];
    const int b = blockIdx.x;
    const int lane = threadIdx.x & 63;
    const int w = __builtin_amdgcn_readfirstlane(threadIdx.x >> 6);
    const int w4 = w & 3;
    const bool hiw = w >= 4;
    const int cls = b & 7;
    const bool dbl = b >= 224;
    const bool moe = (b >= 80) && (b < 224);
    const bool fin = (b >= 8) && (b < 80);
    const int kt = hiw ? (b >> 3) + 4 : (b >> 3);
    const bool p2act = !hiw || dbl;
    const u32 lbase = (u32)(uintptr_t)smem;
    const __amdgpu_buffer_rsrc_t rs = __builtin_amdgcn_make_buffer_rsrc(ws, 0, WS_BYTES, 0x00020000);
    STAMP(0);
    const u64 tstart = __builtin_amdgcn_s_memrealtime();

    f2 xv[12];
    f2 wr2[6];
    const int l48 = lane < 48 ? lane : 47;
    if (!hiw) {
        const u16* xa = x + (size_t)(b + 256 * w4) * 576 + l48;
        const u16* xb = xa + 4 * 256 * 576;
#pragma unroll
        for (int i = 0; i < 12; ++i) { xv[i].x = bfl(xa[48 * i]); xv[i].y = bfl(xb[48 * i]); }
    }
    const u64 epoch = __hip_atomic_load((const u64*)(ws + WS_EPOCH), __ATOMIC_RELAXED, __HIP_MEMORY_SCOPE_AGENT);
    if (hiw) {
        const int q = w4 * 64 + lane;
        const int k1 = 8 * q + cls;
        const int sh = (cls & 1) * 16;
#pragma unroll
        for (int tb = 0; tb < 2; ++tb) {
            if (tb == 1 && !dbl) break;
            const int k0 = ((b >> 3) + 4 * tb) * 8;
            const int eo = (k1 * 289 + k0) & ~1;
            const u32* pr = (const u32*)(awr + eo);
            const u32* pi = (const u32*)(awi + eo);
            u32 a[5], c[5];
#pragma unroll
            for (int i = 0; i < 5; ++i) { a[i] = pr[i]; c[i] = pi[i]; }
            LAS u32x2* dst = (LAS u32x2*)(smem + (tb ? L_WSTB : L_WST)) + q;
#pragma unroll
            for (int i = 0; i < 4; ++i) {
                u32 rr = __builtin_amdgcn_alignbit(a[i + 1], a[i], sh), ii = __builtin_amdgcn_alignbit(c[i + 1], c[i], sh);
                dst[i * 256] = u32x2{(rr & 0xFFFFu) | (ii << 16), (rr >> 16) | (ii & 0xFFFF0000u)};
            }
        }
        if (b < 8) {
            const int km = (2048 - k1) & 2047;
            f2 A = f2{bfl(awr[k1 * 289]), bfl(awi[k1 * 289])}, B = f2{bfl(awr[km * 289]), bfl(awi[km * 289])};
            f2 C = f2{bfl(awr[k1 * 289 + 288]), bfl(awi[k1 * 289 + 288])}, Dd = f2{bfl(awr[km * 289 + 288]), bfl(awi[km * 289 + 288])};
            f2 h0 = add_conj(A, B), hn = add_conj(C, Dd);
            const float sc = 0.5f / (2048.f * 576.f) * 0.125f;
            f2 al, be;
            if ((cls & 3) == 0) { al = (h0 + hn) * sc; be = (h0 - hn) * sc; }
            else { al = (cls < 4 ? h0 : hn) * sc; be = f2{0.f, 0.f}; }
            *(LAS f4*)(smem + L_S0 + q * 16) = f4{al.x, al.y, be.x, be.y};
        }
        if (moe) {
            const int s = ((b - 80) & 7) * 18 + ((b - 80) >> 3);
            const u16* gsrc = wg + (size_t)(lane >> 2) * 4608 + s * 32 + (lane & 3) * 8;
            const long du = wu - wg;
            const u16* dsrc = wd + (size_t)s * 32 * 576 + lane * 8;
#pragma unroll 1
            for (int pc = w4; pc < N1_DMA; pc += 4) {
                const u16* src = pc < 36 ? gsrc + (size_t)pc * 16 * 4608 : pc < 72 ? gsrc + du + (size_t)(pc - 36) * 16 * 4608 : dsrc + (pc - 72) * 512;
                glds16(src, lbase + L_BIG + pc * 1024);
            }
        }
    } else {
#pragma unroll
        for (int i = 0; i < 6; ++i) wr2[i] = f2{bfl(bwr[l48 + 96 * i]), bfl(bwr[l48 + 96 * i + 48])};
    }
    TwA<12> tw1, tw2;
    if (!hiw) { twfill(tw1, l48, 1.f / 576.f); twfill(tw2, l48 & 3, 1.f / 48.f); }
    const float cwa = (float)(b * (lane & 7)) * (1.f / 2048.f);
    const float cwc = __builtin_amdgcn_cosf(cwa), cws = __builtin_amdgcn_sinf(cwa);
    auto ctw = [&](int r) { const float c = rdl(cwc, r), q = rdl(cws, r); return Tw{f2{c, c}, f2{q, -q}}; };
    u64 T;
    {
        u64 z = (u64)(u32)__builtin_amdgcn_readfirstlane((u32)epoch) | ((u64)(u32)__builtin_amdgcn_readfirstlane((u32)(epoch >> 32)) << 32);
        z = (z + 1) * 0x9E3779B97F4A7C15ull;
        z = (z ^ (z >> 30)) * 0xBF58476D1CE4E5B9ull; z = (z ^ (z >> 27)) * 0x94D049BB133111EBull; T = z ^ (z >> 31);
    }
    const u32 t0 = (u32)T & 511u, t1 = (u32)(T >> 9) & 511u, t2 = (u32)(T >> 18) & 511u, t3 = (u32)(T >> 27) & 511u;
    const u32 tagA = (u32)T, tagB = (u32)(T >> 32);
    Tg g0{512u - t0, t0}, g1{512u - t1, t1}, g2{512u - t2, t2}, g3{512u - t3, t3};
    asm volatile("" : "+s"(g0.c), "+s"(g1.c), "+s"(g2.c), "+s"(g3.c), "+v"(g0.t), "+v"(g1.t), "+v"(g2.t), "+v"(g3.t));
    float wsum_r = 0.f;

    LAS f2* const Zw = (LAS f2*)(smem + L_Z) + w4 * 624;
    if (!hiw) {
        {
            float s = 0.f;
#pragma unroll
            for (int i = 0; i < 6; ++i) s += wr2[i].x + wr2[i].y;
            wsum_r = wsum(lane < 48 ? s : 0.f);
        }
        STAMP(1);
        const f2 c2 = f2{rdl(xv[0].x, 0), rdl(xv[0].y, 0)};
        f2 v[12];
#pragma unroll
        for (int i = 0; i < 12; ++i) v[i] = xv[i] - c2;
        f2 s1, s2;
        {
            f2 a[3], q[3];
#pragma unroll
            for (int i = 0; i < 3; ++i) { a[i] = v[i] + v[i + 3]; q[i] = v[i] * v[i]; }
#pragma unroll
            for (int i = 3; i < 12; ++i) { if (i >= 6) a[i % 3] += v[i]; q[i % 3] = pfma(v[i], v[i], q[i % 3]); }
            s1 = a[0] + a[1] + a[2]; s2 = q[0] + q[1] + q[2];
        }
        if (lane >= 48) { s1 = f2{0.f, 0.f}; s2 = f2{0.f, 0.f}; }
        s1 = f2{wsum(s1.x), wsum(s1.y)}; s2 = f2{wsum(s2.x), wsum(s2.y)};
        const f2 md = s1 * (1.f / 576.f);
        f2 var = pfma(-md, md, s2 * (1.f / 576.f));
        var = __builtin_elementwise_max(var, f2{0.f, 0.f});
        const f2 m = rbf2(c2 + md);
        const f2 ve = rbf2(rbf2(var) + 1e-5f);
        const f2 sd = rbf2(f2{__builtin_amdgcn_sqrtf(ve.x), __builtin_amdgcn_sqrtf(ve.y)});
        const f2 rsd = f2{__builtin_amdgcn_rcpf(sd.x), __builtin_amdgcn_rcpf(sd.y)};
        const f2 off = (c2 - m) * rsd;
        if (lane < 48) {
#pragma unroll
            for (int i = 0; i < 12; ++i) v[i] = sumdiff(pfma(v[i], rsd, off));
            r12<1>(v);
#pragma unroll
            for (int i = 1; i < 12; ++i) v[i] = cmul(v[i], tw1[i]);
#pragma unroll
            for (int i = 0; i < 12; ++i) Zw[i * 52 + lane] = v[i];
            CFENCE();
#pragma unroll
            for (int i = 0; i < 12; ++i) v[i] = Zw[(lane >> 2) * 52 + 4 * i + (lane & 3)];
            r12<1>(v);
#pragma unroll
            for (int i = 1; i < 12; ++i) v[i] = cmul(v[i], tw2[i]);
            CFENCE();
#pragma unroll
            for (int i = 0; i < 12; ++i) Zw[(lane & 3) * 148 + (lane >> 2) + 12 * i] = v[i];
            CFENCE();
#pragma unroll
            for (int g = 0; g < 3; ++g)
#pragma unroll
                for (int c = 0; c < 4; ++c) v[g * 4 + c] = Zw[c * 148 + lane + 48 * g];
#pragma unroll
            for (int g = 0; g < 3; ++g) r4<1>(v[g * 4], v[g * 4 + 1], v[g * 4 + 2], v[g * 4 + 3]);
            {
                f2 P = add_pi(v[0], v[2]), Q = add_mi(v[0], v[2]);
                if (lane == 0) { v[0] = P; v[2] = Q; }
            }
            CFENCE();
            Zw[lane == 0 ? 577 : lane + 289] = v[2];
#pragma unroll
            for (int g = 0; g < 3; ++g) {
                Zw[lane + 48 * g] = v[g * 4]; Zw[lane + 48 * g + 144] = v[g * 4 + 1];
                if (g) Zw[lane + 48 * g + 289] = v[g * 4 + 2];
                Zw[lane + 48 * g + 433] = v[g * 4 + 3];
            }
        }
        STAMP(2);
    }
    bar();
    if (lane < 36) {
        const int h = w & 1, kap = (w >> 1) * 36 + lane;
        f2 u0[4], u1[4];
#pragma unroll
        for (int a = 0; a < 4; ++a) {
            const LAS f2* Za = (const LAS f2*)(smem + L_Z) + a * 624;
            f4 zp = *(const LAS f4*)(Za + 2 * kap), zm = *(const LAS f4*)(Za + 576 - 2 * kap);
            if (h == 0) { u0[a] = add_conj(zp.xy, zm.zw); u1[a] = add_conj(zp.zw, zm.xy); }
            else { u0[a] = odd_sep(zp.xy, zm.zw); u1[a] = odd_sep(zp.zw, zm.xy); }
        }
        if (h == 0) { r4<1>(u0[0], u0[1], u0[2], u0[3]); r4<1>(u1[0], u1[1], u1[2], u1[3]); }
        else { r4odd<1>(u0[0], u0[1], u0[2], u0[3]); r4odd<1>(u1[0], u1[1], u1[2], u1[3]); }
        if (w < 2) {
            if (h == 0) {
                f2 a1 = add_conj(u0[1], u0[3]), a3 = odd_sep(u0[3], u0[1]);
                if (lane == 0) { u0[1] = a1; u0[3] = a3; }
            } else {
                f2 a0 = add_conj(u0[0], u0[3]), a3 = odd_sep(u0[3], u0[0]), a1 = add_conj(u0[1], u0[2]), a2 = odd_sep(u0[2], u0[1]);
                if (lane == 0) { u0[0] = a0; u0[1] = a1; u0[2] = a2; u0[3] = a3; }
            }
        }
        const u32 o = WS_E1 + (((h * 36 + (kap >> 2)) * 256 + b) * 64 + (kap & 3) * 16);
#pragma unroll
        for (int rp = 0; rp < 4; ++rp) {
            const Tw cw = ctw(2 * rp + h);
            const f2 a = cmul(u0[rp], cw), c = cmul(u1[rp], cw);
            u32x4 g = {tagd(a.x, g0), tagd(a.y, g1), tagd(c.x, g2), tagd(c.y, g3)};
            __builtin_amdgcn_raw_buffer_store_b128(g, rs, o + rp * (72 * 256 * 64), 0, 16);
        }
    }
    STAMP(3);
    LAS f2* const TT = (LAS f2*)(smem + (hiw ? L_TB : L_TA));
    const int mp = lane >> 4, mt = (lane >> 1) & 7, me = lane & 1;
    const int jj = (lane >> 2) + 16 * w4, kp = lane & 3;
    TwA<8> H; f2 Hb[8];
    TwA<4> tw256; TwA<8> tw64;
    const bool s0w = (b < 8) && (w == 0);
    if (p2act) {
        const LAS u32* wst = (const LAS u32*)(smem + (hiw ? L_WSTB : L_WST)) + w4 * 512 + (4 * mt + mp) * 2 + me;
        const float sc = 0.5f / (2048.f * 576.f);
#pragma unroll
        for (int d = 0; d < 8; ++d) { const f2 hv = unbf(wst[d * 64]) * sc; H.set(d, hv.x, f2{-hv.y, hv.y}); }
        if (s0w) {
#pragma unroll
            for (int d = 0; d < 8; ++d) {
                f4 s = *(const LAS f4*)(smem + L_S0 + (32 * d + 4 * mt + mp) * 16);
                if (me == 0) H.set(d, s.x, f2{-s.y, s.y});
                Hb[d] = me == 0 ? s.zw : f2{0.f, 0.f};
            }
        }
        H.pin(); asm volatile("" : "+v"(H.n[0]));
        twfill(tw256, jj, 1.f / 256.f); twfill(tw64, mt, 1.f / 64.f);
        vm0();
        u32x4 g[4];
        const u32 o = WS_E1 + ((cls * 36 + kt) * 256 + jj) * 64 + kp * 16;
        u32 pend = 15u;
        for (int spin = 0; spin < SPIN_CAP; ++spin) {
#pragma unroll
            for (int i = 0; i < 4; ++i) if (pend >> i & 1) g[i] = __builtin_amdgcn_raw_buffer_load_b128(rs, o, i * 4096, 16);
#pragma unroll
            for (int i = 0; i < 4; ++i) if (pend >> i & 1) {
                u32 bad = ((g[i].x ^ t0) | (g[i].y ^ t1) | (g[i].z ^ t2) | (g[i].w ^ t3)) & 1023u;
                if (__builtin_amdgcn_ballot_w64(bad != 0) == 0) pend &= ~(1u << i);
            }
            if (!pend) break;
            __builtin_amdgcn_s_sleep(1); CFENCE();
        }
        STAMP(4);
        f2 a[4], c[4];
#pragma unroll
        for (int i = 0; i < 4; ++i) { a[i] = f2{__uint_as_float(g[i].x), __uint_as_float(g[i].y)}; c[i] = f2{__uint_as_float(g[i].z), __uint_as_float(g[i].w)}; }
        r4<1>(a[0], a[1], a[2], a[3]); r4<1>(c[0], c[1], c[2], c[3]);
#pragma unroll
        for (int p = 0; p < 4; ++p) {
            if (p) { a[p] = cmul(a[p], tw256[p]); c[p] = cmul(c[p], tw256[p]); }
            *(LAS f4*)(TT + kp * 612 + p * 144 + jj * 2) = f4{a[p].x, a[p].y, c[p].x, c[p].y};
        }
    }
    bar();
    if (hiw && moe) {
        const int s = ((b - 80) & 7) * 18 + ((b - 80) >> 3);
        const u16* gsrc = wg + (size_t)(lane >> 2) * 4608 + s * 32 + (lane & 3) * 8;
        const long du = wu - wg;
        const u16* dsrc = wd + (size_t)s * 32 * 576 + lane * 8;
#pragma unroll 1
        for (int pc = N1_DMA + w4; pc < 108; pc += 4) {
            const u16* src = pc < 36 ? gsrc + (size_t)pc * 16 * 4608 : pc < 72 ? gsrc + du + (size_t)(pc - 36) * 16 * 4608 : dsrc + (pc - 72) * 512;
            glds16(src, lbase + L_BIG + pc * 1024);
        }
    }
    if (p2act) {
        f2 v[8];
        const LAS f2* rd = TT + w4 * 612 + mp * 144 + mt * 2 + me;
#pragma unroll
        for (int i = 0; i < 8; ++i) v[i] = rd[16 * i];
        r8<1>(v);
#pragma unroll
        for (int c = 1; c < 8; ++c) v[c] = cmul(v[c], tw64[c]);
        LAS f2* bx = TT + w4 * 608 + mp * 130 + me * 8;
        CFENCE();
#pragma unroll
        for (int c = 0; c < 8; ++c) bx[c * 16 + mt] = v[c];
        CFENCE();
#pragma unroll
        for (int t = 0; t < 8; t += 2) { f4 z = *(const LAS f4*)(bx + mt * 16 + t); v[t] = z.xy; v[t + 1] = z.zw; }
        r8<1>(v);
        if (s0w && (cls & 3) == 0) {
            LAS f2* ex = (LAS f2*)(smem + L_S0 + 4096);
            const int q0 = 4 * mt + mp;
            if (me == 0) {
#pragma unroll
                for (int d = 0; d < 8; ++d) ex[32 * d + q0] = v[d];
            }
            CFENCE();
#pragma unroll
            for (int d = 0; d < 8; ++d) {
                const int q = 32 * d + q0;
                f2 xm = ex[cls == 0 ? (256 - q) & 255 : 255 - q];
                f2 t = cmul(v[d], H[d]);
                f2 u = cmulc(Hb[d], mktw(xm));
                v[d] = t + u;
            }
        } else {
#pragma unroll
            for (int d = 0; d < 8; ++d) v[d] = cmul(v[d], H[d]);
        }
        r8<-1>(v);
#pragma unroll
        for (int c = 1; c < 8; ++c) v[c] = cmulc(v[c], tw64[c]);
        CFENCE();
#pragma unroll
        for (int c = 0; c < 8; ++c) bx[c * 16 + mt] = v[c];
        CFENCE();
#pragma unroll
        for (int t = 0; t < 8; t += 2) { f4 z = *(const LAS f4*)(bx + mt * 16 + t); v[t] = z.xy; v[t + 1] = z.zw; }
        r8<-1>(v);
        CFENCE();
        LAS f2* wrp = TT + w4 * 616 + mp * 144 + mt * 2 + me;
#pragma unroll
        for (int i = 0; i < 8; ++i) wrp[16 * i] = v[i];
    }
    bar();
    if (p2act) {
        f2 a[4], c[4];
#pragma unroll
        for (int p = 0; p < 4; ++p) {
            f4 z = *(const LAS f4*)(TT + kp * 616 + p * 144 + jj * 2);
            a[p] = z.xy; c[p] = z.zw;
            if (p) { a[p] = cmulc(a[p], tw256[p]); c[p] = cmulc(c[p], tw256[p]); }
        }
        r4<-1>(a[0], a[1], a[2], a[3]); r4<-1>(c[0], c[1], c[2], c[3]);
        const u32 o = WS_E2 + jj * 18432 + (cls * 36 + kt) * 64 + kp * 16;
#pragma unroll
        for (int i = 0; i < 4; ++i) {
            u32x4 g = {tagd(a[i].x, g0), tagd(a[i].y, g1), tagd(c[i].x, g2), tagd(c[i].y, g3)};
            __builtin_amdgcn_raw_buffer_store_b128(g, rs, o + i * 64 * 18432, 0, 16);
        }
    }
    STAMP(5);
    TwA<12> tv1, tv2;
    if (!hiw) { int lr = l48; asm volatile("" : "+v"(lr)); twfill(tv1, lr, 1.f / 576.f); twfill(tv2, lr & 3, 1.f / 48.f); }
    vm0();
    if (lane < 36) {
        const int k = w * 36 + lane;
        const u32 ta = (lane & 1) ? t2 : t0, tb = (lane & 1) ? t3 : t1;
        const u32 o = WS_E2 + b * 18432 + k * 8;
        u32x2 g[8];
        u32 pend = 255u;
        for (int spin = 0; spin < SPIN_CAP; ++spin) {
#pragma unroll
            for (int r = 0; r < 8; ++r) if (pend >> r & 1) g[r] = __builtin_amdgcn_raw_buffer_load_b64(rs, o + r * 2304, 0, 16);
#pragma unroll
            for (int r = 0; r < 8; ++r) if (pend >> r & 1) {
                u32 bad = ((g[r].x ^ ta) | (g[r].y ^ tb)) & 1023u;
                if (__builtin_amdgcn_ballot_w64(bad != 0) == 0) pend &= ~(1u << r);
            }
            if (!pend) break;
            __builtin_amdgcn_s_sleep(1); CFENCE();
        }
        STAMP(6);
        f2 v[8];
#pragma unroll
        for (int r = 0; r < 8; ++r) { v[r] = f2{__uint_as_float(g[r].x), __uint_as_float(g[r].y)}; if (r) v[r] = cmulc(v[r], ctw(r)); }
        if (w == 0) {
            f2 c2 = v[2] + v[6].yx, c6 = (v[2] - v[6].yx) * PM;
            f2 c1 = v[1] + v[7].yx, c7 = (v[1] - v[7].yx) * PM;
            f2 c3 = v[3] + v[5].yx, c5 = (v[3] - v[5].yx) * PM;
            if (lane == 0) { v[2] = c2; v[6] = c6; v[1] = c1; v[7] = c7; v[3] = c3; v[5] = c5; }
        }
        r4<-1>(v[0], v[2], v[4], v[6]);
        r4<-1>(v[1], v[3], v[5], v[7]);
        LAS f2* Z0 = (LAS f2*)(smem + L_Z);
        const f2 hh = f2{C_H, C_H};
        Z0[k] = add_pi(v[0], v[1]); Z0[577 - k] = add_mi(v[0], v[1]);
        { f2 t = nadd_pi(v[3], v[3]); Z0[624 + k] = pfma(t, hh, v[2]); Z0[624 + 577 - k] = pfma(t, -hh, v[2]); }
        Z0[2 * 624 + k] = v[4] - v[5]; Z0[2 * 624 + 577 - k] = v[4] + v[5];
        { f2 t = nadd_mi(v[7], v[7]); Z0[3 * 624 + k] = pfma(t, hh, v[6]); Z0[3 * 624 + 577 - k] = pfma(t, -hh, v[6]); }
    }
    bar();
    u32 po[12], ph[12], pl[12];
    s16x4 fa[18], fd[10];
    const int tq = (lane & 15) >> 2, tp = lane & 3, tg = lane >> 4;
    auto load_frags = [&]() {
        const u32 mb = lbase + ((w & 2) ? L_WU : L_WG) + (w & 1) * 32 + tp * 8;
#pragma unroll
        for (int s = 0; s < 9; ++s)
#pragma unroll
            for (int t = 0; t < 2; ++t) {
                const int p = 288 * (w >> 2) + 32 * s + 8 * tg + 4 * t + tq;
                const int row = p / 12 + 48 * (p % 12);
                fa[s * 2 + t] = __builtin_bit_cast(s16x4, __builtin_amdgcn_ds_read_tr16_b64_v4i16((LAS s16x4*)(uintptr_t)(mb + row * 64)));
            }
#pragma unroll
        for (int i = 0; i < 5; ++i) {
            const int ct = (i < 4) ? w + 8 * i : 32 + w4;
#pragma unroll
            for (int t = 0; t < 2; ++t)
                fd[i * 2 + t] = __builtin_bit_cast(s16x4, __builtin_amdgcn_ds_read_tr16_b64_v4i16((LAS s16x4*)(uintptr_t)(lbase + L_WD + (8 * tg + 4 * t + tq) * 1152 + ct * 32 + tp * 8)));
        }
    };
    if (!hiw) {
        f2 v[12];
        if (lane < 48) {
            v[2] = Zw[lane == 0 ? 577 : lane + 289];
#pragma unroll
            for (int g = 0; g < 3; ++g) {
                v[g * 4] = Zw[lane + 48 * g]; v[g * 4 + 1] = Zw[lane + 48 * g + 144];
                if (g) v[g * 4 + 2] = Zw[lane + 48 * g + 289];
                v[g * 4 + 3] = Zw[lane + 48 * g + 433];
            }
            {
                f2 z0 = add_conj(v[0], v[2]), d = sub_conj(v[0], v[2]);
                if (lane == 0) { v[0] = z0; v[2] = d.yx; }
            }
#pragma unroll
            for (int g = 0; g < 3; ++g) {
                f2 s0 = add_conj(v[g * 4], v[g * 4 + 2]), s1 = sub_conj(v[g * 4], v[g * 4 + 2]);
                f2 s2 = add_conj(v[g * 4 + 1], v[g * 4 + 3]), s3 = sub_conj(v[g * 4 + 1], v[g * 4 + 3]);
                v[g * 4] = s0 + s2; v[g * 4 + 2] = s0 - s2; v[g * 4 + 1] = add_pi(s1, s3); v[g * 4 + 3] = add_mi(s1, s3);
            }
            CFENCE();
#pragma unroll
            for (int g = 0; g < 3; ++g)
#pragma unroll
                for (int c = 0; c < 4; ++c) Zw[c * 152 + lane + 48 * g] = v[g * 4 + c];
            CFENCE();
#pragma unroll
            for (int i = 0; i < 12; ++i) v[i] = Zw[(lane & 3) * 152 + (lane >> 2) + 12 * i];
#pragma unroll
            for (int i = 1; i < 12; ++i) v[i] = cmulc(v[i], tv2[i]);
            r12<-1>(v);
            CFENCE();
#pragma unroll
            for (int i = 0; i < 12; ++i) Zw[(lane >> 2) * 52 + 4 * i + (lane & 3)] = v[i];
            CFENCE();
#pragma unroll
            for (int i = 0; i < 12; ++i) v[i] = Zw[i * 52 + lane];
#pragma unroll
            for (int i = 1; i < 12; ++i) v[i] = cmulc(v[i], tv1[i]);
            r12<-1>(v);
        }
        STAMP(7);
#pragma unroll
        for (int i = 0; i < 12; ++i) { ph[i] = pkbf(xv[i] + rbf2(sumdiff(v[i]))); v[i] = unbf(ph[i]); }
        const f2 c2 = f2{rdl(v[0].x, 0), rdl(v[0].y, 0)};
        f2 s1, s2, s3;
        {
            f2 d[12], a[2], q[2], c[2];
#pragma unroll
            for (int i = 0; i < 12; ++i) d[i] = v[i] - c2;
#pragma unroll
            for (int i = 0; i < 2; ++i) { a[i] = d[i] + d[i + 2]; q[i] = d[i] * d[i]; c[i] = d[i] * (i ? wr2[0].yy : wr2[0].xx); }
#pragma unroll
            for (int i = 2; i < 12; ++i) { if (i >= 4) a[i & 1] += d[i]; q[i & 1] = pfma(d[i], d[i], q[i & 1]); c[i & 1] = pfma(d[i], (i & 1) ? wr2[i / 2].yy : wr2[i / 2].xx, c[i & 1]); }
            s1 = a[0] + a[1]; s2 = q[0] + q[1]; s3 = c[0] + c[1];
        }
        if (lane >= 48) { s1 = f2{0.f, 0.f}; s2 = f2{0.f, 0.f}; s3 = f2{0.f, 0.f}; }
        s1 = f2{wsum(s1.x), wsum(s1.y)}; s2 = f2{wsum(s2.x), wsum(s2.y)}; s3 = f2{wsum(s3.x), wsum(s3.y)};
        const f2 md = s1 * (1.f / 576.f);
        f2 var = pfma(-md, md, s2 * (1.f / 576.f));
        var = __builtin_elementwise_max(var, f2{0.f, 0.f});
        const f2 m = rbf2(c2 + md);
        const f2 ve = rbf2(rbf2(var) + 1e-5f);
        const f2 sd = rbf2(f2{__builtin_amdgcn_sqrtf(ve.x), __builtin_amdgcn_sqrtf(ve.y)});
        const f2 rsd = f2{__builtin_amdgcn_rcpf(sd.x), __builtin_amdgcn_rcpf(sd.y)};
        const u32 rb = pkbf(pfma(c2 - m, f2{wsum_r, wsum_r}, s3) * rsd);
        if (lane < 2) {
            u32 bits = lane ? rb >> 16 : rb & 0xFFFFu;
            u32 key = (bits & 0x8000u) ? (bits ^ 0xFFFFu) : (bits | 0x8000u);
            if ((bits & 0x7FFFu) > 0x7F80u) key = 0;
            const u32 row = b + 256 * (w4 + 4 * lane);
            ((LAS u32*)(smem + L_KEYS))[w4 + 4 * lane] = (key << 16) | (0xFFFFu - row);
        }
#pragma unroll
        for (int i = 0; i < 12; ++i) {
            pl[i] = pkbf(rbf2(v[i] - m) * rsd);
            po[i] = pkbf(v[i] + unbf(pl[i]));
        }
    } else if (moe) {
        load_frags();
    }
    bar();
    STAMP(8);
    u32 bk1, bk2;
    {
        const u32x4 ka = *(const LAS u32x4*)(smem + L_KEYS), kb = *(const LAS u32x4*)(smem + L_KEYS + 16);
        u32 k[8] = {ka.x, ka.y, ka.z, ka.w, kb.x, kb.y, kb.z, kb.w};
        bk1 = 0; bk2 = 0;
#pragma unroll
        for (int i = 0; i < 8; ++i) { u32 lo = k[i] < bk1 ? k[i] : bk1; bk1 = k[i] > bk1 ? k[i] : bk1; bk2 = lo > bk2 ? lo : bk2; }
        bk1 = __builtin_amdgcn_readfirstlane(bk1); bk2 = __builtin_amdgcn_readfirstlane(bk2);
    }
    auto publish_row = [&](const u32* pk, int hi, u32 off) {
        if (lane < 48) {
            u32 q[6];
#pragma unroll
            for (int i = 0; i < 6; ++i) q[i] = hi ? ((pk[2 * i] >> 16) | (pk[2 * i + 1] & 0xFFFF0000u)) : ((pk[2 * i] & 0xFFFFu) | (pk[2 * i + 1] << 16));
            __builtin_amdgcn_raw_buffer_store_b128(u32x4{q[0], q[1], q[2], tagA}, rs, off + lane * 32, 0, 16);
            __builtin_amdgcn_raw_buffer_store_b128(u32x4{q[3], q[4], q[5], tagA}, rs, off + lane * 32 + 16, 0, 16);
        }
    };
    if (!hiw) {
#pragma unroll
        for (int hi = 0; hi < 2; ++hi) {
            const u32 row = b + 256 * (w4 + 4 * hi);
            if (((0xFFFFu - row) == (bk1 & 0xFFFFu)) || ((0xFFFFu - row) == (bk2 & 0xFFFFu))) publish_row(pl, hi, WS_ROWL + row * 1536);
        }
        if (moe) load_frags();
    } else if (w == 4) {
        if (lane == 0) __builtin_amdgcn_raw_buffer_store_b128(u32x4{bk1, bk2, tagA, tagB}, rs, WS_CAND + b * 16, 0, 16);
        u32 m1 = 0, m2 = 0;
        for (int spin = 0; spin < SPIN_CAP; ++spin) {
            u32x4 g[4];
#pragma unroll
            for (int i = 0; i < 4; ++i) g[i] = __builtin_amdgcn_raw_buffer_load_b128(rs, WS_CAND + lane * 16, i * 1024, 16);
            u32 bad = 0;
#pragma unroll
            for (int i = 0; i < 4; ++i) bad |= (g[i].z ^ tagA) | (g[i].w ^ tagB);
            m1 = 0; m2 = 0;
#pragma unroll
            for (int i = 0; i < 4; ++i) {
                u32 lo = g[i].x < m1 ? g[i].x : m1; m1 = g[i].x > m1 ? g[i].x : m1;
                lo = g[i].y > lo ? g[i].y : lo; m2 = lo > m2 ? lo : m2;
            }
            if (__builtin_amdgcn_ballot_w64(bad != 0) == 0) break;
            __builtin_amdgcn_s_sleep(1); CFENCE();
        }
        const u32 K1 = wmax(m1);
        const u32 K2 = wmax(m1 == K1 ? m2 : m1);
        STAMP(9);
        if (lane == 0) *(LAS u32x2*)(smem + L_TOP) = u32x2{K1, K2};
        if (moe) {
            const u32 r1 = (0xFFFFu - (K1 & 0xFFFFu)) & 2047u, r2 = (0xFFFFu - (K2 & 0xFFFFu)) & 2047u;
            u32x4 g[3];
            u32 of[3], ld[3];
#pragma unroll
            for (int i = 0; i < 3; ++i) { const u32 n = lane + 64 * i; const bool s = n >= 96; of[i] = WS_ROWL + (s ? r2 : r1) * 1536 + (s ? n - 96 : n) * 16; ld[i] = L_XS + n * 12; }
            for (int spin = 0; spin < SPIN_CAP; ++spin) {
                u32 bad = 0;
#pragma unroll
                for (int i = 0; i < 3; ++i) g[i] = __builtin_amdgcn_raw_buffer_load_b128(rs, of[i], 0, 16);
#pragma unroll
                for (int i = 0; i < 3; ++i) bad |= g[i].w ^ tagA;
                if (__builtin_amdgcn_ballot_w64(bad != 0) == 0) break;
                __builtin_amdgcn_s_sleep(1); CFENCE();
            }
#pragma unroll
            for (int i = 0; i < 3; ++i) { LAS u32* d = (LAS u32*)(smem + ld[i]); d[0] = g[i].x; d[1] = g[i].y; d[2] = g[i].z; }
        }
        STAMP(10);
    }
    bar();
    if (b == 0 && threadIdx.x == 448) __hip_atomic_store((u64*)(ws + WS_EPOCH), epoch + 1, __ATOMIC_RELAXED, __HIP_MEMORY_SCOPE_AGENT);
    const u32x2 top = *(const LAS u32x2*)(smem + L_TOP);
    const u32 K1 = __builtin_amdgcn_readfirstlane(top.x), K2 = __builtin_amdgcn_readfirstlane(top.y);
    const u32 row1 = (0xFFFFu - (K1 & 0xFFFFu)) & 2047u, row2 = (0xFFFFu - (K2 & 0xFFFFu)) & 2047u;
    if (!hiw) {
#pragma unroll
        for (int hi = 0; hi < 2; ++hi) {
            const u32 row = b + 256 * (w4 + 4 * hi);
            if (row == row1) publish_row(ph, hi, WS_ROWH);
            if (row == row2) publish_row(ph, hi, WS_ROWH + 1536);
        }
    }
    if (moe) {
        const int s = ((b - 80) & 7) * 18 + ((b - 80) >> 3);
        f4 acc = {0.f, 0.f, 0.f, 0.f};
        const LAS char* xs = (const LAS char*)(smem + L_XS) + (lane & 1) * 1152 + (w >> 2) * 576 + tg * 16;
#pragma unroll
        for (int st = 0; st < 9; ++st) {
            const bf8 bfr = *(const LAS bf8*)(xs + st * 64);
            const s16x8 af = __builtin_shufflevector(fa[2 * st], fa[2 * st + 1], 0, 1, 2, 3, 4, 5, 6, 7);
            acc = __builtin_amdgcn_mfma_f32_16x16x32_bf16(__builtin_bit_cast(bf8, af), bfr, acc, 0, 0, 0);
        }
        if ((lane & 15) < 2) *(LAS f4*)(smem + L_GU + w * 128 + (lane & 1) * 64 + tg * 16) = acc;
        bar();
        {
            const int tok = lane >> 5, f = lane & 31;
            const LAS float* gu = (const LAS float*)(smem + L_GU) + (f >> 4) * 32 + tok * 16 + (f & 15);
            const float g = rbf(gu[0] + gu[4 * 32]), u = rbf(gu[2 * 32] + gu[6 * 32]);
            const float sg = rbf(g * __builtin_amdgcn_rcpf(1.f + __builtin_amdgcn_exp2f(-1.4426950408889634f * g)));
            const u32 ab = pkbf(f2{sg * u, 0.f});
            ((LAS u16*)(smem + L_AB + w * 128))[lane] = (u16)ab;
        }
        CFENCE();
        const bf8 bfr = *(const LAS bf8*)(smem + L_AB + w * 128 + (lane & 1) * 64 + tg * 16);
#pragma unroll
        for (int i = 0; i < 5; ++i) {
            if (i == 4 && hiw) break;
            const int ct = (i < 4) ? w + 8 * i : 32 + w4;
            const s16x8 af = __builtin_shufflevector(fd[2 * i], fd[2 * i + 1], 0, 1, 2, 3, 4, 5, 6, 7);
            const f4 d = __builtin_amdgcn_mfma_f32_16x16x32_bf16(__builtin_bit_cast(bf8, af), bfr, f4{0.f, 0.f, 0.f, 0.f}, 0, 0, 0);
            if ((lane & 15) < 2) {
                u32x4 g = {tagd(d.x, g0), tagd(d.y, g1), tagd(d.z, g2), tagd(d.w, g3)};
                __builtin_amdgcn_raw_buffer_store_b128(g, rs, WS_PB + (((lane & 1) * 36 + ct) * 144 + s) * 64 + tg * 16, 0, 16);
            }
        }
        STAMP(11);
    }
    if (!hiw) {
        if (lane < 48) {
#pragma unroll
            for (int hi = 0; hi < 2; ++hi) {
                const u32 row = b + 256 * (w4 + 4 * hi);
                if (row != row1 && row != row2) {
                    u16* o = out + (size_t)row * 576 + lane;
#pragma unroll
                    for (int i = 0; i < 12; ++i) __hip_atomic_store(&o[48 * i], (u16)(hi ? po[i] >> 16 : po[i]), __ATOMIC_RELAXED, __HIP_MEMORY_SCOPE_AGENT);
                }
            }
        }
    } else if (fin && w == 5) {
        const int ch = b - 8, tok = ch / 36, ct = ch % 36;
        const u32 row = tok ? row2 : row1, K = tok ? K2 : K1;
        const int c0 = ct * 16, ii = c0 / 48;
        const u32 go = (2 * (c0 % 48 + (lane & 15)) + (ii >= 6 ? 1 : 0)) * 16;
        const int e = ii % 6;
        u32x4 gl, gh;
        for (int spin = 0; spin < SPIN_CAP; ++spin) {
            gl = __builtin_amdgcn_raw_buffer_load_b128(rs, WS_ROWL + row * 1536 + go, 0, 16);
            gh = __builtin_amdgcn_raw_buffer_load_b128(rs, WS_ROWH + tok * 1536 + go, 0, 16);
            if (__builtin_amdgcn_ballot_w64(((gl.w ^ tagA) | (gh.w ^ tagA)) != 0) == 0) break;
            __builtin_amdgcn_s_sleep(2); CFENCE();
        }
        const u32 dl = e < 2 ? gl.x : e < 4 ? gl.y : gl.z, dh = e < 2 ? gh.x : e < 4 ? gh.y : gh.z;
        const float lv = (e & 1) ? bfh(dl) : bfl(dl), hv = (e & 1) ? bfh(dh) : bfl(dh);
        const u32 kb = K >> 16;
        const float rv = bfl((kb & 0x8000u) ? (kb & 0x7FFFu) : (kb ^ 0xFFFFu));
        const float rsel = rbf(__builtin_amdgcn_rcpf(1.f + __builtin_amdgcn_exp2f(-1.4426950408889634f * rv)));
        u32x4 g[9];
        const u32 o = WS_PB + ch * 9216 + lane * 16;
        u32 pend = 511u;
        for (int spin = 0; spin < SPIN_CAP; ++spin) {
#pragma unroll
            for (int i = 0; i < 9; ++i) if (pend >> i & 1) g[i] = __builtin_amdgcn_raw_buffer_load_b128(rs, o, i * 1024, 16);
#pragma unroll
            for (int i = 0; i < 9; ++i) if (pend >> i & 1) {
                u32 bad = ((g[i].x ^ t0) | (g[i].y ^ t1) | (g[i].z ^ t2) | (g[i].w ^ t3)) & 1023u;
                if (__builtin_amdgcn_ballot_w64(bad != 0) == 0) pend &= ~(1u << i);
            }
            if (!pend) break;
            __builtin_amdgcn_s_sleep(2); CFENCE();
        }
        STAMP(12);
        f4 a = {0.f, 0.f, 0.f, 0.f};
#pragma unroll
        for (int i = 0; i < 9; ++i) a += __builtin_bit_cast(f4, g[i]);
        float r[4] = {a.x, a.y, a.z, a.w};
#pragma unroll
        for (int i = 0; i < 4; ++i) {
            r[i] = dppa<0x124, 0xF>(r[i]);
            r[i] = dppa<0x128, 0xF>(r[i]);
            r[i] += __shfl_xor(r[i], 16); r[i] += __shfl_xor(r[i], 32);
        }
        const int src = (lane & 15) >> 2;
        float y = 0.f;
#pragma unroll
        for (int i = 0; i < 4; ++i) { float t = __shfl(r[i], src); if ((lane & 3) == i) y = t; }
        const float upd = rbf(rbf(y) * rsel);
        const u32 ob = pkbf(f2{hv + rbf(lv + upd), 0.f});
        if (lane < 16) __hip_atomic_store(&out[(size_t)row * 576 + c0 + lane], (u16)ob, __ATOMIC_RELAXED, __HIP_MEMORY_SCOPE_AGENT);
        STAMP(13);
    }
    while (__builtin_amdgcn_s_memrealtime() < tstart + PAD_US * 100) __builtin_amdgcn_s_sleep(4);
    STAMP(14);
}

extern "C" void kernel_launch(void* const* d_in, const int* in_sizes, int n_in, void* d_out, int out_size, void* d_ws, size_t ws_size,
                              hipStream_t stream) {
    (void)in_sizes; (void)n_in; (void)out_size; (void)ws_size;
    (void)hipFuncSetAttribute((const void*)fused_kernel, hipFuncAttributeMaxDynamicSharedMemorySize, L_TOTAL);
    fused_kernel<<<256, 512, L_TOTAL, stream>>>((const u16*)d_in[0], (const u16*)d_in[1], (const u16*)d_in[2], (const u16*)d_in[4],
                                               (const u16*)d_in[5], (const u16*)d_in[6], (const u16*)d_in[7], (u16*)d_out, (char*)d_ws);
}
```

```cpp
#define PAD_US 40
#include <hip/hip_runtime.h>
#include <stdint.h>

typedef unsigned short u16;
typedef unsigned int u32;
typedef unsigned long long u64;
typedef float f2 __attribute__((ext_vector_type(2)));
typedef float f4 __attribute__((ext_vector_type(4)));
typedef u32 u32x2 __attribute__((ext_vector_type(2)));
typedef u32 u32x4 __attribute__((ext_vector_type(4)));
typedef __bf16 bf2 __attribute__((ext_vector_type(2)));
typedef __bf16 bf8 __attribute__((ext_vector_type(8)));
typedef short s16x4 __attribute__((ext_vector_type(4)));
typedef short s16x8 __attribute__((ext_vector_type(8)));
#define DEV __device__ __forceinline__
#define LAS __attribute__((address_space(3)))

#define N1_DMA 56
#define SPIN_CAP 400000

#define WS_EPOCH   0u
#define WS_CAND    4096u
#define WS_ROWH    8192u
#define WS_PB      16384u
#define WS_ROWL    (1u << 20)
#define WS_E1      (5u << 20)
#define WS_E2      (10u << 20)
#define WS_BYTES   (15u << 20)
#define WS_STAMP   (16u << 20)
#define L_Z        0u
#define L_TA       19968u
#define L_WST      39488u
#define L_SP       47680u
#define L_KEYS     (L_SP)
#define L_TOP      (L_SP + 64u)
#define L_XS       (L_SP + 128u)
#define L_GU       (L_SP + 2560u)
#define L_AB       (L_SP + 3584u)
#define L_BIG      52288u
#define L_WG       (L_BIG)
#define L_WU       (L_BIG + 36864u)
#define L_WD       (L_BIG + 73728u)
#define L_TB       (L_BIG)
#define L_WSTB     (L_BIG + 19520u)
#define L_S0       (L_BIG + 32768u)
#define L_TOTAL    (L_BIG + 110592u)

DEV f2 pfma(f2 a, f2 b, f2 c) { return __builtin_elementwise_fma(a, b, c); }
#define PM f2{1.f, -1.f}
#define MP f2{-1.f, 1.f}
DEV f2 add_mi(f2 a, f2 b) { return pfma(b.yx, PM, a); }
DEV f2 add_pi(f2 a, f2 b) { return pfma(b.yx, MP, a); }
DEV f2 nadd_mi(f2 a, f2 b) { return pfma(b.yx, PM, -a); }
DEV f2 nadd_pi(f2 a, f2 b) { return pfma(b.yx, MP, -a); }
DEV f2 add_conj(f2 a, f2 b) { return pfma(b, PM, a); }
DEV f2 sub_conj(f2 a, f2 b) { return pfma(b, MP, a); }
DEV f2 odd_sep(f2 a, f2 b) { return pfma(a.yx, PM, b.yx); }
DEV f2 sumdiff(f2 a) { return pfma(a.yy, PM, a.xx); }
struct Tw { f2 x; f2 n; };
DEV Tw mktw(f2 w) { return Tw{w.xx, f2{-w.y, w.y}}; }
DEV f2 cmul(f2 a, Tw w) { return pfma(a.yx, w.n, a * w.x); }
DEV f2 cmulc(f2 a, Tw w) { return pfma(a.yx, -w.n, a * w.x); }
template<int N> struct TwA {
    f2 xp[(N + 1) / 2]; f2 n[N];
    DEV Tw operator[](int i) const { return Tw{(i & 1) ? xp[i / 2].yy : xp[i / 2].xx, n[i]}; }
    DEV void set(int i, float x, f2 nn) { if (i & 1) xp[i / 2].y = x; else xp[i / 2].x = x; n[i] = nn; }
    DEV void pin() {
#pragma unroll
        for (int i = 0; i < (N + 1) / 2; ++i) asm volatile("" : "+v"(xp[i]));
#pragma unroll
        for (int i = 1; i < N; ++i) asm volatile("" : "+v"(n[i]));
    }
};
template<int DIR> DEV f2 addr_(f2 a, f2 b) { return DIR > 0 ? add_mi(a, b) : add_pi(a, b); }
template<int DIR> DEV f2 subr_(f2 a, f2 b) { return DIR > 0 ? add_pi(a, b) : add_mi(a, b); }
template<int DIR> DEV f2 naddr_(f2 a, f2 b) { return DIR > 0 ? nadd_mi(a, b) : nadd_pi(a, b); }

#define C_H 0.70710678118654752440f
#define C_K 0.86602540378443864676f
template<int DIR> DEV void r4(f2& a, f2& b, f2& c, f2& d) {
    f2 s0 = a + c, s1 = a - c, s2 = b + d, s3 = b - d;
    a = s0 + s2; c = s0 - s2; b = addr_<DIR>(s1, s3); d = subr_<DIR>(s1, s3);
}
template<int DIR> DEV void r3(f2& a, f2& b, f2& c) {
    f2 t = b + c, d = b - c;
    f2 m = pfma(t, f2{-0.5f, -0.5f}, a);
    a = a + t;
    const f2 kk = DIR > 0 ? f2{C_K, -C_K} : f2{-C_K, C_K};
    b = pfma(d.yx, kk, m);
    c = pfma(d.yx, -kk, m);
}
template<int DIR> DEV void r4odd(f2& a, f2& b, f2& c, f2& d) {
    f2 o0 = addr_<DIR>(a, c), o2 = subr_<DIR>(a, c);
    f2 p = addr_<DIR>(b, d), m = subr_<DIR>(b, d);
    f2 tp = addr_<DIR>(p, p), tm = naddr_<DIR>(m, m);
    const f2 hh = f2{C_H, C_H};
    a = pfma(tp, hh, o0); c = pfma(tp, -hh, o0); b = pfma(tm, hh, o2); d = pfma(tm, -hh, o2);
}
template<int DIR> DEV void r8(f2* v) {
    f2 a0 = v[0] + v[4], a4 = v[0] - v[4], a1 = v[1] + v[5], a5 = v[1] - v[5];
    f2 a2 = v[2] + v[6], a6 = v[2] - v[6], a3 = v[3] + v[7], a7 = v[3] - v[7];
    r4<DIR>(a0, a1, a2, a3);
    r4odd<DIR>(a4, a5, a6, a7);
    v[0] = a0; v[2] = a1; v[4] = a2; v[6] = a3; v[1] = a4; v[3] = a5; v[5] = a6; v[7] = a7;
}
template<int DIR> DEV void r12(f2* v) {
#pragma unroll
    for (int a = 0; a < 3; ++a) r4<DIR>(v[(4 * a) % 12], v[(4 * a + 3) % 12], v[(4 * a + 6) % 12], v[(4 * a + 9) % 12]);
#pragma unroll
    for (int b = 0; b < 4; ++b) r3<DIR>(v[(3 * b) % 12], v[(4 + 3 * b) % 12], v[(8 + 3 * b) % 12]);
    f2 o[12];
#pragma unroll
    for (int a = 0; a < 3; ++a)
#pragma unroll
        for (int b = 0; b < 4; ++b) o[(4 * a + 9 * b) % 12] = v[(4 * a + 3 * b) % 12];
#pragma unroll
    for (int i = 0; i < 12; ++i) v[i] = o[i];
}
template<int N> DEV void twfill(TwA<N>& t, int base, float invN) {
    const float st = (float)base * invN;
    t.xp[0].x = 1.f;
#pragma unroll
    for (int i = 1; i < N; ++i) { const float x = st * (float)i; const float sn = __builtin_amdgcn_sinf(x); t.set(i, __builtin_amdgcn_cosf(x), f2{sn, -sn}); }
    t.pin();
}

DEV float bfl(u32 u) { return __uint_as_float(u << 16); }
DEV float bfh(u32 u) { return __uint_as_float(u & 0xFFFF0000u); }
DEV u32 pkbf(f2 v) { bf2 b = __builtin_convertvector(v, bf2); return __builtin_bit_cast(u32, b); }
DEV f2 unbf(u32 u) { return f2{bfl(u), bfh(u)}; }
DEV f2 rbf2(f2 v) { return unbf(pkbf(v)); }
DEV float rbf(float v) { return bfl(pkbf(f2{v, v})); }

template<int CTRL, int RM> DEV float dppa(float v) { return v + __int_as_float(__builtin_amdgcn_update_dpp(0, __float_as_int(v), CTRL, RM, 0xF, false)); }
DEV float wsum(float v) {
    v = dppa<0x111, 0xF>(v); v = dppa<0x112, 0xF>(v); v = dppa<0x114, 0xF>(v); v = dppa<0x118, 0xF>(v);
    v = dppa<0x142, 0xA>(v); v = dppa<0x143, 0xC>(v);
    return __int_as_float(__builtin_amdgcn_readlane(__float_as_int(v), 63));
}
template<int CTRL, int RM> DEV u32 dppm(u32 v) { u32 o = (u32)__builtin_amdgcn_update_dpp(0, (int)v, CTRL, RM, 0xF, false); return v > o ? v : o; }
DEV u32 wmax(u32 v) {
    v = dppm<0x111, 0xF>(v); v = dppm<0x112, 0xF>(v); v = dppm<0x114, 0xF>(v); v = dppm<0x118, 0xF>(v);
    v = dppm<0x142, 0xA>(v); v = dppm<0x143, 0xC>(v);
    return (u32)__builtin_amdgcn_readlane((int)v, 63);
}
DEV float rdl(float v, int l) { return __int_as_float(__builtin_amdgcn_readlane(__float_as_int(v), l)); }

#define CFENCE() asm volatile("" ::: "memory")
DEV void bar() { asm volatile("s_waitcnt lgkmcnt(0)\n\ts_barrier" ::: "memory"); }
DEV void vm0() { asm volatile("s_waitcnt vmcnt(0)" ::: "memory"); }
DEV void glds16(const void* gsrc, u32 lds_dst) {
    u32 keep;
    asm volatile("s_mov_b32 %0, m0\n\ts_mov_b32 m0, %2\n\ts_nop 0\n\tglobal_load_lds_dwordx4 %1, off nt\n\ts_mov_b32 m0, %0" : "=&s"(keep) : "v"(gsrc), "s"(lds_dst) : "memory");
}
struct Tg { u32 c, t; };
DEV u32 tagd(float v, Tg g) { return ((__float_as_uint(v) + g.c) & 0xFFFFFC00u) | g.t; }

#define STAMP(i) do {} while (0)

__global__ void __launch_bounds__(512, 2) fused_kernel(const u16* __restrict__ x, const u16* __restrict__ awr, const u16* __restrict__ awi,
                                                       const u16* __restrict__ bwr, const u16* __restrict__ wg, const u16* __restrict__ wu,
                                                       const u16* __restrict__ wd, u16* out, char* ws) {
    extern __shared__ __attribute__((aligned(16))) char smem[];
    const int b = blockIdx.x;
    const int lane = threadIdx.x & 63;
    const int w = __builtin_amdgcn_readfirstlane(threadIdx.x >> 6);
    const int w4 = w & 3;
    const bool hiw = w >= 4;
    const int cls = b & 7;
    const bool dbl = b >= 224;
    const bool moe = (b >= 80) && (b < 224);
    const bool fin = (b >= 8) && (b < 80);
    const int kt = hiw ? (b >> 3) + 4 : (b >> 3);
    const bool p2act = !hiw || dbl;
    const u32 lbase = (u32)(uintptr_t)smem;
    const __amdgpu_buffer_rsrc_t rs = __builtin_amdgcn_make_buffer_rsrc(ws, 0, WS_BYTES, 0x00020000);
    STAMP(0);
    const u64 tstart = __builtin_amdgcn_s_memrealtime();

    f2 xv[12];
    f2 wr2[6];
    const int l48 = lane < 48 ? lane : 47;
    if (!hiw) {
        const u16* xa = x + (size_t)(b + 256 * w4) * 576 + l48;
        const u16* xb = xa + 4 * 256 * 576;
#pragma unroll
        for (int i = 0; i < 12; ++i) { xv[i].x = bfl(xa[48 * i]); xv[i].y = bfl(xb[48 * i]); }
    }
    const u64 epoch = __hip_atomic_load((const u64*)(ws + WS_EPOCH), __ATOMIC_RELAXED, __HIP_MEMORY_SCOPE_AGENT);
    if (hiw) {
        const int q = w4 * 64 + lane;
        const int k1 = 8 * q + cls;
        const int sh = (cls & 1) * 16;
#pragma unroll
        for (int tb = 0; tb < 2; ++tb) {
            if (tb == 1 && !dbl) break;
            const int k0 = ((b >> 3) + 4 * tb) * 8;
            const int eo = (k1 * 289 + k0) & ~1;
            const u32* pr = (const u32*)(awr + eo);
            const u32* pi = (const u32*)(awi + eo);
            u32 a[5], c[5];
#pragma unroll
            for (int i = 0; i < 5; ++i) { a[i] = pr[i]; c[i] = pi[i]; }
            LAS u32x2* dst = (LAS u32x2*)(smem + (tb ? L_WSTB : L_WST)) + q;
#pragma unroll
            for (int i = 0; i < 4; ++i) {
                u32 rr = __builtin_amdgcn_alignbit(a[i + 1], a[i], sh), ii = __builtin_amdgcn_alignbit(c[i + 1], c[i], sh);
                dst[i * 256] = u32x2{(rr & 0xFFFFu) | (ii << 16), (rr >> 16) | (ii & 0xFFFF0000u)};
            }
        }
        if (b < 8) {
            const int km = (2048 - k1) & 2047;
            f2 A = f2{bfl(awr[k1 * 289]), bfl(awi[k1 * 289])}, B = f2{bfl(awr[km * 289]), bfl(awi[km * 289])};
            f2 C = f2{bfl(awr[k1 * 289 + 288]), bfl(awi[k1 * 289 + 288])}, Dd = f2{bfl(awr[km * 289 + 288]), bfl(awi[km * 289 + 288])};
            f2 h0 = add_conj(A, B), hn = add_conj(C, Dd);
            const float sc = 0.5f / (2048.f * 576.f) * 0.125f;
            f2 al, be;
            if ((cls & 3) == 0) { al = (h0 + hn) * sc; be = (h0 - hn) * sc; }
            else { al = (cls < 4 ? h0 : hn) * sc; be = f2{0.f, 0.f}; }
            *(LAS f4*)(smem + L_S0 + q * 16) = f4{al.x, al.y, be.x, be.y};
        }
        if (moe) {
            const int s = ((b - 80) & 7) * 18 + ((b - 80) >> 3);
            const u16* gsrc = wg + (size_t)(lane >> 2) * 4608 + s * 32 + (lane & 3) * 8;
            const long du = wu - wg;
            const u16* dsrc = wd + (size_t)s * 32 * 576 + lane * 8;
#pragma unroll 1
            for (int pc = w4; pc < N1_DMA; pc += 4) {
                const u16* src = pc < 36 ? gsrc + (size_t)pc * 16 * 4608 : pc < 72 ? gsrc + du + (size_t)(pc - 36) * 16 * 4608 : dsrc + (pc - 72) * 512;
                glds16(src, lbase + L_BIG + pc * 1024);
            }
        }
    } else {
#pragma unroll
        for (int i = 0; i < 6; ++i) wr2[i] = f2{bfl(bwr[l48 + 96 * i]), bfl(bwr[l48 + 96 * i + 48])};
    }
    TwA<12> tw1, tw2;
    if (!hiw) { twfill(tw1, l48, 1.f / 576.f); twfill(tw2, l48 & 3, 1.f / 48.f); }
    const float cwa = (float)(b * (lane & 7)) * (1.f / 2048.f);
    const float cwc = __builtin_amdgcn_cosf(cwa), cws = __builtin_amdgcn_sinf(cwa);
    auto ctw = [&](int r) { const float c = rdl(cwc, r), q = rdl(cws, r); return Tw{f2{c, c}, f2{q, -q}}; };
    u64 T;
    {
        u64 z = (u64)(u32)__builtin_amdgcn_readfirstlane((u32)epoch) | ((u64)(u32)__builtin_amdgcn_readfirstlane((u32)(epoch >> 32)) << 32);
        z = (z + 1) * 0x9E3779B97F4A7C15ull;
        z = (z ^ (z >> 30)) * 0xBF58476D1CE4E5B9ull; z = (z ^ (z >> 27)) * 0x94D049BB133111EBull; T = z ^ (z >> 31);
    }
    const u32 t0 = (u32)T & 511u, t1 = (u32)(T >> 9) & 511u, t2 = (u32)(T >> 18) & 511u, t3 = (u32)(T >> 27) & 511u;
    const u32 tagA = (u32)T, tagB = (u32)(T >> 32);
    Tg g0{512u - t0, t0}, g1{512u - t1, t1}, g2{512u - t2, t2}, g3{512u - t3, t3};
    asm volatile("" : "+s"(g0.c), "+s"(g1.c), "+s"(g2.c), "+s"(g3.c), "+v"(g0.t), "+v"(g1.t), "+v"(g2.t), "+v"(g3.t));
    float wsum_r = 0.f;

    LAS f2* const Zw = (LAS f2*)(smem + L_Z) + w4 * 624;
    if (!hiw) {
        {
            float s = 0.f;
#pragma unroll
            for (int i = 0; i < 6; ++i) s += wr2[i].x + wr2[i].y;
            wsum_r = wsum(lane < 48 ? s : 0.f);
        }
        STAMP(1);
        const f2 c2 = f2{rdl(xv[0].x, 0), rdl(xv[0].y, 0)};
        f2 v[12];
#pragma unroll
        for (int i = 0; i < 12; ++i) v[i] = xv[i] - c2;
        f2 s1, s2;
        {
            f2 a[3], q[3];
#pragma unroll
            for (int i = 0; i < 3; ++i) { a[i] = v[i] + v[i + 3]; q[i] = v[i] * v[i]; }
#pragma unroll
            for (int i = 3; i < 12; ++i) { if (i >= 6) a[i % 3] += v[i]; q[i % 3] = pfma(v[i], v[i], q[i % 3]); }
            s1 = a[0] + a[1] + a[2]; s2 = q[0] + q[1] + q[2];
        }
        if (lane >= 48) { s1 = f2{0.f, 0.f}; s2 = f2{0.f, 0.f}; }
        s1 = f2{wsum(s1.x), wsum(s1.y)}; s2 = f2{wsum(s2.x), wsum(s2.y)};
        const f2 md = s1 * (1.f / 576.f);
        f2 var = pfma(-md, md, s2 * (1.f / 576.f));
        var = __builtin_elementwise_max(var, f2{0.f, 0.f});
        const f2 m = rbf2(c2 + md);
        const f2 ve = rbf2(rbf2(var) + 1e-5f);
        const f2 sd = rbf2(f2{__builtin_amdgcn_sqrtf(ve.x), __builtin_amdgcn_sqrtf(ve.y)});
        const f2 rsd = f2{__builtin_amdgcn_rcpf(sd.x), __builtin_amdgcn_rcpf(sd.y)};
        const f2 off = (c2 - m) * rsd;
        if (lane < 48) {
#pragma unroll
            for (int i = 0; i < 12; ++i) v[i] = sumdiff(pfma(v[i], rsd, off));
            r12<1>(v);
#pragma unroll
            for (int i = 1; i < 12; ++i) v[i] = cmul(v[i], tw1[i]);
#pragma unroll
            for (int i = 0; i < 12; ++i) Zw[i * 52 + lane] = v[i];
            CFENCE();
#pragma unroll
            for (int i = 0; i < 12; ++i) v[i] = Zw[(lane >> 2) * 52 + 4 * i + (lane & 3)];
            r12<1>(v);
#pragma unroll
            for (int i = 1; i < 12; ++i) v[i] = cmul(v[i], tw2[i]);
            CFENCE();
#pragma unroll
            for (int i = 0; i < 12; ++i) Zw[(lane & 3) * 148 + (lane >> 2) + 12 * i] = v[i];
            CFENCE();
#pragma unroll
            for (int g = 0; g < 3; ++g)
#pragma unroll
                for (int c = 0; c < 4; ++c) v[g * 4 + c] = Zw[c * 148 + lane + 48 * g];
#pragma unroll
            for (int g = 0; g < 3; ++g) r4<1>(v[g * 4], v[g * 4 + 1], v[g * 4 + 2], v[g * 4 + 3]);
            {
                f2 P = add_pi(v[0], v[2]), Q = add_mi(v[0], v[2]);
                if (lane == 0) { v[0] = P; v[2] = Q; }
            }
            CFENCE();
            Zw[lane == 0 ? 577 : lane + 289] = v[2];
#pragma unroll
            for (int g = 0; g < 3; ++g) {
                Zw[lane + 48 * g] = v[g * 4]; Zw[lane + 48 * g + 144] = v[g * 4 + 1];
                if (g) Zw[lane + 48 * g + 289] = v[g * 4 + 2];
                Zw[lane + 48 * g + 433] = v[g * 4 + 3];
            }
        }
        STAMP(2);
    }
    bar();
    if (lane < 36) {
        const int h = w & 1, kap = (w >> 1) * 36 + lane;
        f2 u0[4], u1[4];
#pragma unroll
        for (int a = 0; a < 4; ++a) {
            const LAS f2* Za = (const LAS f2*)(smem + L_Z) + a * 624;
            f4 zp = *(const LAS f4*)(Za + 2 * kap), zm = *(const LAS f4*)(Za + 576 - 2 * kap);
            if (h == 0) { u0[a] = add_conj(zp.xy, zm.zw); u1[a] = add_conj(zp.zw, zm.xy); }
            else { u0[a] = odd_sep(zp.xy, zm.zw); u1[a] = odd_sep(zp.zw, zm.xy); }
        }
        if (h == 0) { r4<1>(u0[0], u0[1], u0[2], u0[3]); r4<1>(u1[0], u1[1], u1[2], u1[3]); }
        else { r4odd<1>(u0[0], u0[1], u0[2], u0[3]); r4odd<1>(u1[0], u1[1], u1[2], u1[3]); }
        if (w < 2) {
            if (h == 0) {
                f2 a1 = add_conj(u0[1], u0[3]), a3 = odd_sep(u0[3], u0[1]);
                if (lane == 0) { u0[1] = a1; u0[3] = a3; }
            } else {
                f2 a0 = add_conj(u0[0], u0[3]), a3 = odd_sep(u0[3], u0[0]), a1 = add_conj(u0[1], u0[2]), a2 = odd_sep(u0[2], u0[1]);
                if (lane == 0) { u0[0] = a0; u0[1] = a1; u0[2] = a2; u0[3] = a3; }
            }
        }
        const u32 o = WS_E1 + (((h * 36 + (kap >> 2)) * 256 + b) * 64 + (kap & 3) * 16);
#pragma unroll
        for (int rp = 0; rp < 4; ++rp) {
            const Tw cw = ctw(2 * rp + h);
            const f2 a = cmul(u0[rp], cw), c = cmul(u1[rp], cw);
            u32x4 g = {tagd(a.x, g0), tagd(a.y, g1), tagd(c.x, g2), tagd(c.y, g3)};
            __builtin_amdgcn_raw_buffer_store_b128(g, rs, o + rp * (72 * 256 * 64), 0, 16);
        }
    }
    STAMP(3);
    LAS f2* const TT = (LAS f2*)(smem + (hiw ? L_TB : L_TA));
    const int mp = lane >> 4, mt = (lane >> 1) & 7, me = lane & 1;
    const int jj = (lane >> 2) + 16 * w4, kp = lane & 3;
    TwA<8> H; f2 Hb[8];
    TwA<4> tw256; TwA<8> tw64;
    const bool s0w = (b < 8) && (w == 0);
    if (p2act) {
        const LAS u32* wst = (const LAS u32*)(smem + (hiw ? L_WSTB : L_WST)) + w4 * 512 + (4 * mt + mp) * 2 + me;
        const float sc = 0.5f / (2048.f * 576.f);
#pragma unroll
        for (int d = 0; d < 8; ++d) { const f2 hv = unbf(wst[d * 64]) * sc; H.set(d, hv.x, f2{-hv.y, hv.y}); }
        if (s0w) {
#pragma unroll
            for (int d = 0; d < 8; ++d) {
                f4 s = *(const LAS f4*)(smem + L_S0 + (32 * d + 4 * mt + mp) * 16);
                if (me == 0) H.set(d, s.x, f2{-s.y, s.y});
                Hb[d] = me == 0 ? s.zw : f2{0.f, 0.f};
            }
        }
        H.pin(); asm volatile("" : "+v"(H.n[0]));
        twfill(tw256, jj, 1.f / 256.f); twfill(tw64, mt, 1.f / 64.f);
        vm0();
        u32x4 g[4];
        const u32 o = WS_E1 + ((cls * 36 + kt) * 256 + jj) * 64 + kp * 16;
        u32 pend = 15u;
        for (int spin = 0; spin < SPIN_CAP; ++spin) {
#pragma unroll
            for (int i = 0; i < 4; ++i) if (pend >> i & 1) g[i] = __builtin_amdgcn_raw_buffer_load_b128(rs, o, i * 4096, 16);
#pragma unroll
            for (int i = 0; i < 4; ++i) if (pend >> i & 1) {
                u32 bad = ((g[i].x ^ t0) | (g[i].y ^ t1) | (g[i].z ^ t2) | (g[i].w ^ t3)) & 1023u;
                if (__builtin_amdgcn_ballot_w64(bad != 0) == 0) pend &= ~(1u << i);
            }
            if (!pend) break;
            __builtin_amdgcn_s_sleep(1); CFENCE();
        }
        STAMP(4);
        f2 a[4], c[4];
#pragma unroll
        for (int i = 0; i < 4; ++i) { a[i] = f2{__uint_as_float(g[i].x), __uint_as_float(g[i].y)}; c[i] = f2{__uint_as_float(g[i].z), __uint_as_float(g[i].w)}; }
        r4<1>(a[0], a[1], a[2], a[3]); r4<1>(c[0], c[1], c[2], c[3]);
#pragma unroll
        for (int p = 0; p < 4; ++p) {
            if (p) { a[p] = cmul(a[p], tw256[p]); c[p] = cmul(c[p], tw256[p]); }
            *(LAS f4*)(TT + kp * 612 + p * 144 + jj * 2) = f4{a[p].x, a[p].y, c[p].x, c[p].y};
        }
    }
    bar();
    if (hiw && moe) {
        const int s = ((b - 80) & 7) * 18 + ((b - 80) >> 3);
        const u16* gsrc = wg + (size_t)(lane >> 2) * 4608 + s * 32 + (lane & 3) * 8;
        const long du = wu - wg;
        const u16* dsrc = wd + (size_t)s * 32 * 576 + lane * 8;
#pragma unroll 1
        for (int pc = N1_DMA + w4; pc < 108; pc += 4) {
            const u16* src = pc < 36 ? gsrc + (size_t)pc * 16 * 4608 : pc < 72 ? gsrc + du + (size_t)(pc - 36) * 16 * 4608 : dsrc + (pc - 72) * 512;
            glds16(src, lbase + L_BIG + pc * 1024);
        }
    }
    if (p2act) {
        f2 v[8];
        const LAS f2* rd = TT + w4 * 612 + mp * 144 + mt * 2 + me;
#pragma unroll
        for (int i = 0; i < 8; ++i) v[i] = rd[16 * i];
        r8<1>(v);
#pragma unroll
        for (int c = 1; c < 8; ++c) v[c] = cmul(v[c], tw64[c]);
        LAS f2* bx = TT + w4 * 608 + mp * 130 + me * 8;
        CFENCE();
#pragma unroll
        for (int c = 0; c < 8; ++c) bx[c * 16 + mt] = v[c];
        CFENCE();
#pragma unroll
        for (int t = 0; t < 8; t += 2) { f4 z = *(const LAS f4*)(bx + mt * 16 + t); v[t] = z.xy; v[t + 1] = z.zw; }
        r8<1>(v);
        if (s0w && (cls & 3) == 0) {
            LAS f2* ex = (LAS f2*)(smem + L_S0 + 4096);
            const int q0 = 4 * mt + mp;
            if (me == 0) {
#pragma unroll
                for (int d = 0; d < 8; ++d) ex[32 * d + q0] = v[d];
            }
            CFENCE();
#pragma unroll
            for (int d = 0; d < 8; ++d) {
                const int q = 32 * d + q0;
                f2 xm = ex[cls == 0 ? (256 - q) & 255 : 255 - q];
                f2 t = cmul(v[d], H[d]);
                f2 u = cmulc(Hb[d], mktw(xm));
                v[d] = t + u;
            }
        } else {
#pragma unroll
            for (int d = 0; d < 8; ++d) v[d] = cmul(v[d], H[d]);
        }
        r8<-1>(v);
#pragma unroll
        for (int c = 1; c < 8; ++c) v[c] = cmulc(v[c], tw64[c]);
        CFENCE();
#pragma unroll
        for (int c = 0; c < 8; ++c) bx[c * 16 + mt] = v[c];
        CFENCE();
#pragma unroll
        for (int t = 0; t < 8; t += 2) { f4 z = *(const LAS f4*)(bx + mt * 16 + t); v[t] = z.xy; v[t + 1] = z.zw; }
        r8<-1>(v);
        CFENCE();
        LAS f2* wrp = TT + w4 * 616 + mp * 144 + mt * 2 + me;
#pragma unroll
        for (int i = 0; i < 8; ++i) wrp[16 * i] = v[i];
    }
    bar();
    if (p2act) {
        f2 a[4], c[4];
#pragma unroll
        for (int p = 0; p < 4; ++p) {
            f4 z = *(const LAS f4*)(TT + kp * 616 + p * 144 + jj * 2);
            a[p] = z.xy; c[p] = z.zw;
            if (p) { a[p] = cmulc(a[p], tw256[p]); c[p] = cmulc(c[p], tw256[p]); }
        }
        r4<-1>(a[0], a[1], a[2], a[3]); r4<-1>(c[0], c[1], c[2], c[3]);
        const u32 o = WS_E2 + jj * 18432 + (cls * 36 + kt) * 64 + kp * 16;
#pragma unroll
        for (int i = 0; i < 4; ++i) {
            u32x4 g = {tagd(a[i].x, g0), tagd(a[i].y, g1), tagd(c[i].x, g2), tagd(c[i].y, g3)};
            __builtin_amdgcn_raw_buffer_store_b128(g, rs, o + i * 64 * 18432, 0, 16);
        }
    }
    STAMP(5);
    TwA<12> tv1, tv2;
    if (!hiw) { int lr = l48; asm volatile("" : "+v"(lr)); twfill(tv1, lr, 1.f / 576.f); twfill(tv2, lr & 3, 1.f / 48.f); }
    vm0();
    if (lane < 36) {
        const int k = w * 36 + lane;
        const u32 ta = (lane & 1) ? t2 : t0, tb = (lane & 1) ? t3 : t1;
        const u32 o = WS_E2 + b * 18432 + k * 8;
        u32x2 g[8];
        u32 pend = 255u;
        for (int spin = 0; spin < SPIN_CAP; ++spin) {
#pragma unroll
            for (int r = 0; r < 8; ++r) if (pend >> r & 1) g[r] = __builtin_amdgcn_raw_buffer_load_b64(rs, o + r * 2304, 0, 16);
#pragma unroll
            for (int r = 0; r < 8; ++r) if (pend >> r & 1) {
                u32 bad = ((g[r].x ^ ta) | (g[r].y ^ tb)) & 1023u;
                if (__builtin_amdgcn_ballot_w64(bad != 0) == 0) pend &= ~(1u << r);
            }
            if (!pend) break;
            __builtin_amdgcn_s_sleep(1); CFENCE();
        }
        STAMP(6);
        f2 v[8];
#pragma unroll
        for (int r = 0; r < 8; ++r) { v[r] = f2{__uint_as_float(g[r].x), __uint_as_float(g[r].y)}; if (r) v[r] = cmulc(v[r], ctw(r)); }
        if (w == 0) {
            f2 c2 = v[2] + v[6].yx, c6 = (v[2] - v[6].yx) * PM;
            f2 c1 = v[1] + v[7].yx, c7 = (v[1] - v[7].yx) * PM;
            f2 c3 = v[3] + v[5].yx, c5 = (v[3] - v[5].yx) * PM;
            if (lane == 0) { v[2] = c2; v[6] = c6; v[1] = c1; v[7] = c7; v[3] = c3; v[5] = c5; }
        }
        r4<-1>(v[0], v[2], v[4], v[6]);
        r4<-1>(v[1], v[3], v[5], v[7]);
        LAS f2* Z0 = (LAS f2*)(smem + L_Z);
        const f2 hh = f2{C_H, C_H};
        Z0[k] = add_pi(v[0], v[1]); Z0[577 - k] = add_mi(v[0], v[1]);
        { f2 t = nadd_pi(v[3], v[3]); Z0[624 + k] = pfma(t, hh, v[2]); Z0[624 + 577 - k] = pfma(t, -hh, v[2]); }
        Z0[2 * 624 + k] = v[4] - v[5]; Z0[2 * 624 + 577 - k] = v[4] + v[5];
        { f2 t = nadd_mi(v[7], v[7]); Z0[3 * 624 + k] = pfma(t, hh, v[6]); Z0[3 * 624 + 577 - k] = pfma(t, -hh, v[6]); }
    }
    bar();
    u32 po[12], ph[12], pl[12];
    s16x4 fa[18], fd[10];
    const int tq = (lane & 15) >> 2, tp = lane & 3, tg = lane >> 4;
    auto load_frags = [&]() {
        const u32 mb = lbase + ((w & 2) ? L_WU : L_WG) + (w & 1) * 32 + tp * 8;
#pragma unroll
        for (int s = 0; s < 9; ++s)
#pragma unroll
            for (int t = 0; t < 2; ++t) {
                const int p = 288 * (w >> 2) + 32 * s + 8 * tg + 4 * t + tq;
                const int row = p / 12 + 48 * (p % 12);
                fa[s * 2 + t] = __builtin_bit_cast(s16x4, __builtin_amdgcn_ds_read_tr16_b64_v4i16((LAS s16x4*)(uintptr_t)(mb + row * 64)));
            }
#pragma unroll
        for (int i = 0; i < 5; ++i) {
            const int ct = (i < 4) ? w + 8 * i : 32 + w4;
#pragma unroll
            for (int t = 0; t < 2; ++t)
                fd[i * 2 + t] = __builtin_bit_cast(s16x4, __builtin_amdgcn_ds_read_tr16_b64_v4i16((LAS s16x4*)(uintptr_t)(lbase + L_WD + (8 * tg + 4 * t + tq) * 1152 + ct * 32 + tp * 8)));
        }
    };
    if (!hiw) {
        f2 v[12];
        if (lane < 48) {
            v[2] = Zw[lane == 0 ? 577 : lane + 289];
#pragma unroll
            for (int g = 0; g < 3; ++g) {
                v[g * 4] = Zw[lane + 48 * g]; v[g * 4 + 1] = Zw[lane + 48 * g + 144];
                if (g) v[g * 4 + 2] = Zw[lane + 48 * g + 289];
                v[g * 4 + 3] = Zw[lane + 48 * g + 433];
            }
            {
                f2 z0 = add_conj(v[0], v[2]), d = sub_conj(v[0], v[2]);
                if (lane == 0) { v[0] = z0; v[2] = d.yx; }
            }
#pragma unroll
            for (int g = 0; g < 3; ++g) {
                f2 s0 = add_conj(v[g * 4], v[g * 4 + 2]), s1 = sub_conj(v[g * 4], v[g * 4 + 2]);
                f2 s2 = add_conj(v[g * 4 + 1], v[g * 4 + 3]), s3 = sub_conj(v[g * 4 + 1], v[g * 4 + 3]);
                v[g * 4] = s0 + s2; v[g * 4 + 2] = s0 - s2; v[g * 4 + 1] = add_pi(s1, s3); v[g * 4 + 3] = add_mi(s1, s3);
            }
            CFENCE();
#pragma unroll
            for (int g = 0; g < 3; ++g)
#pragma unroll
                for (int c = 0; c < 4; ++c) Zw[c * 152 + lane + 48 * g] = v[g * 4 + c];
            CFENCE();
#pragma unroll
            for (int i = 0; i < 12; ++i) v[i] = Zw[(lane & 3) * 152 + (lane >> 2) + 12 * i];
#pragma unroll
            for (int i = 1; i < 12; ++i) v[i] = cmulc(v[i], tv2[i]);
            r12<-1>(v);
            CFENCE();
#pragma unroll
            for (int i = 0; i < 12; ++i) Zw[(lane >> 2) * 52 + 4 * i + (lane & 3)] = v[i];
            CFENCE();
#pragma unroll
            for (int i = 0; i < 12; ++i) v[i] = Zw[i * 52 + lane];
#pragma unroll
            for (int i = 1; i < 12; ++i) v[i] = cmulc(v[i], tv1[i]);
            r12<-1>(v);
        }
        STAMP(7);
#pragma unroll
        for (int i = 0; i < 12; ++i) { ph[i] = pkbf(xv[i] + rbf2(sumdiff(v[i]))); v[i] = unbf(ph[i]); }
        const f2 c2 = f2{rdl(v[0].x, 0), rdl(v[0].y, 0)};
        f2 s1, s2, s3;
        {
            f2 d[12], a[2], q[2], c[2];
#pragma unroll
            for (int i = 0; i < 12; ++i) d[i] = v[i] - c2;
#pragma unroll
            for (int i = 0; i < 2; ++i) { a[i] = d[i] + d[i + 2]; q[i] = d[i] * d[i]; c[i] = d[i] * (i ? wr2[0].yy : wr2[0].xx); }
#pragma unroll
            for (int i = 2; i < 12; ++i) { if (i >= 4) a[i & 1] += d[i]; q[i & 1] = pfma(d[i], d[i], q[i & 1]); c[i & 1] = pfma(d[i], (i & 1) ? wr2[i / 2].yy : wr2[i / 2].xx, c[i & 1]); }
            s1 = a[0] + a[1]; s2 = q[0] + q[1]; s3 = c[0] + c[1];
        }
        if (lane >= 48) { s1 = f2{0.f, 0.f}; s2 = f2{0.f, 0.f}; s3 = f2{0.f, 0.f}; }
        s1 = f2{wsum(s1.x), wsum(s1.y)}; s2 = f2{wsum(s2.x), wsum(s2.y)}; s3 = f2{wsum(s3.x), wsum(s3.y)};
        const f2 md = s1 * (1.f / 576.f);
        f2 var = pfma(-md, md, s2 * (1.f / 576.f));
        var = __builtin_elementwise_max(var, f2{0.f, 0.f});
        const f2 m = rbf2(c2 + md);
        const f2 ve = rbf2(rbf2(var) + 1e-5f);
        const f2 sd = rbf2(f2{__builtin_amdgcn_sqrtf(ve.x), __builtin_amdgcn_sqrtf(ve.y)});
        const f2 rsd = f2{__builtin_amdgcn_rcpf(sd.x), __builtin_amdgcn_rcpf(sd.y)};
        const u32 rb = pkbf(pfma(c2 - m, f2{wsum_r, wsum_r}, s3) * rsd);
        if (lane < 2) {
            u32 bits = lane ? rb >> 16 : rb & 0xFFFFu;
            u32 key = (bits & 0x8000u) ? (bits ^ 0xFFFFu) : (bits | 0x8000u);
            if ((bits & 0x7FFFu) > 0x7F80u) key = 0;
            const u32 row = b + 256 * (w4 + 4 * lane);
            ((LAS u32*)(smem + L_KEYS))[w4 + 4 * lane] = (key << 16) | (0xFFFFu - row);
        }
#pragma unroll
        for (int i = 0; i < 12; ++i) {
            pl[i] = pkbf(rbf2(v[i] - m) * rsd);
            po[i] = pkbf(v[i] + unbf(pl[i]));
        }
    } else if (moe) {
        load_frags();
    }
    bar();
    STAMP(8);
    u32 bk1, bk2;
    {
        const u32x4 ka = *(const LAS u32x4*)(smem + L_KEYS), kb = *(const LAS u32x4*)(smem + L_KEYS + 16);
        u32 k[8] = {ka.x, ka.y, ka.z, ka.w, kb.x, kb.y, kb.z, kb.w};
        bk1 = 0; bk2 = 0;
#pragma unroll
        for (int i = 0; i < 8; ++i) { u32 lo = k[i] < bk1 ? k[i] : bk1; bk1 = k[i] > bk1 ? k[i] : bk1; bk2 = lo > bk2 ? lo : bk2; }
        bk1 = __builtin_amdgcn_readfirstlane(bk1); bk2 = __builtin_amdgcn_readfirstlane(bk2);
    }
    auto publish_row = [&](const u32* pk, int hi, u32 off) {
        if (lane < 48) {
            u32 q[6];
#pragma unroll
            for (int i = 0; i < 6; ++i) q[i] = hi ? ((pk[2 * i] >> 16) | (pk[2 * i + 1] & 0xFFFF0000u)) : ((pk[2 * i] & 0xFFFFu) | (pk[2 * i + 1] << 16));
            __builtin_amdgcn_raw_buffer_store_b128(u32x4{q[0], q[1], q[2], tagA}, rs, off + lane * 32, 0, 16);
            __builtin_amdgcn_raw_buffer_store_b128(u32x4{q[3], q[4], q[5], tagA}, rs, off + lane * 32 + 16, 0, 16);
        }
    };
    if (!hiw) {
#pragma unroll
        for (int hi = 0; hi < 2; ++hi) {
            const u32 row = b + 256 * (w4 + 4 * hi);
            if (((0xFFFFu - row) == (bk1 & 0xFFFFu)) || ((0xFFFFu - row) == (bk2 & 0xFFFFu))) publish_row(pl, hi, WS_ROWL + row * 1536);
        }
        if (moe) load_frags();
    } else if (w == 4) {
        if (lane == 0) __builtin_amdgcn_raw_buffer_store_b128(u32x4{bk1, bk2, tagA, (u32)tstart}, rs, WS_CAND + b * 16, 0, 16);
        u32 m1 = 0, m2 = 0, tmx = 0;
        for (int spin = 0; spin < SPIN_CAP; ++spin) {
            u32x4 g[4];
#pragma unroll
            for (int i = 0; i < 4; ++i) g[i] = __builtin_amdgcn_raw_buffer_load_b128(rs, WS_CAND + lane * 16, i * 1024, 16);
            u32 bad = 0; tmx = 0;
#pragma unroll
            for (int i = 0; i < 4; ++i) { bad |= (g[i].z ^ tagA); u32 dd = (u32)tstart + 100000u - g[i].w; tmx = dd > tmx ? dd : tmx; }
            m1 = 0; m2 = 0;
#pragma unroll
            for (int i = 0; i < 4; ++i) {
                u32 lo = g[i].x < m1 ? g[i].x : m1; m1 = g[i].x > m1 ? g[i].x : m1;
                lo = g[i].y > lo ? g[i].y : lo; m2 = lo > m2 ? lo : m2;
            }
            if (__builtin_amdgcn_ballot_w64(bad != 0) == 0) break;
            __builtin_amdgcn_s_sleep(1); CFENCE();
        }
        const u32 K1 = wmax(m1);
        const u32 K2 = wmax(m1 == K1 ? m2 : m1);
        STAMP(9);
        const u32 TM = wmax(tmx);
        if (lane == 0) { *(LAS u32x2*)(smem + L_TOP) = u32x2{K1, K2}; *(LAS u64*)(smem + L_TOP + 8) = tstart + 100000u - TM; }
        if (moe) {
            const u32 r1 = (0xFFFFu - (K1 & 0xFFFFu)) & 2047u, r2 = (0xFFFFu - (K2 & 0xFFFFu)) & 2047u;
            u32x4 g[3];
            u32 of[3], ld[3];
#pragma unroll
            for (int i = 0; i < 3; ++i) { const u32 n = lane + 64 * i; const bool s = n >= 96; of[i] = WS_ROWL + (s ? r2 : r1) * 1536 + (s ? n - 96 : n) * 16; ld[i] = L_XS + n * 12; }
            for (int spin = 0; spin < SPIN_CAP; ++spin) {
                u32 bad = 0;
#pragma unroll
                for (int i = 0; i < 3; ++i) g[i] = __builtin_amdgcn_raw_buffer_load_b128(rs, of[i], 0, 16);
#pragma unroll
                for (int i = 0; i < 3; ++i) bad |= g[i].w ^ tagA;
                if (__builtin_amdgcn_ballot_w64(bad != 0) == 0) break;
                __builtin_amdgcn_s_sleep(1); CFENCE();
            }
#pragma unroll
            for (int i = 0; i < 3; ++i) { LAS u32* d = (LAS u32*)(smem + ld[i]); d[0] = g[i].x; d[1] = g[i].y; d[2] = g[i].z; }
        }
        STAMP(10);
    }
    bar();
    if (b == 0 && threadIdx.x == 448) __hip_atomic_store((u64*)(ws + WS_EPOCH), epoch + 1, __ATOMIC_RELAXED, __HIP_MEMORY_SCOPE_AGENT);
    const u32x2 top = *(const LAS u32x2*)(smem + L_TOP);
    const u32 K1 = __builtin_amdgcn_readfirstlane(top.x), K2 = __builtin_amdgcn_readfirstlane(top.y);
    const u32 row1 = (0xFFFFu - (K1 & 0xFFFFu)) & 2047u, row2 = (0xFFFFu - (K2 & 0xFFFFu)) & 2047u;
    if (!hiw) {
#pragma unroll
        for (int hi = 0; hi < 2; ++hi) {
            const u32 row = b + 256 * (w4 + 4 * hi);
            if (row == row1) publish_row(ph, hi, WS_ROWH);
            if (row == row2) publish_row(ph, hi, WS_ROWH + 1536);
        }
    }
    if (moe) {
        const int s = ((b - 80) & 7) * 18 + ((b - 80) >> 3);
        f4 acc = {0.f, 0.f, 0.f, 0.f};
        const LAS char* xs = (const LAS char*)(smem + L_XS) + (lane & 1) * 1152 + (w >> 2) * 576 + tg * 16;
#pragma unroll
        for (int st = 0; st < 9; ++st) {
            const bf8 bfr = *(const LAS bf8*)(xs + st * 64);
            const s16x8 af = __builtin_shufflevector(fa[2 * st], fa[2 * st + 1], 0, 1, 2, 3, 4, 5, 6, 7);
            acc = __builtin_amdgcn_mfma_f32_16x16x32_bf16(__builtin_bit_cast(bf8, af), bfr, acc, 0, 0, 0);
        }
        if ((lane & 15) < 2) *(LAS f4*)(smem + L_GU + w * 128 + (lane & 1) * 64 + tg * 16) = acc;
        bar();
        {
            const int tok = lane >> 5, f = lane & 31;
            const LAS float* gu = (const LAS float*)(smem + L_GU) + (f >> 4) * 32 + tok * 16 + (f & 15);
            const float g = rbf(gu[0] + gu[4 * 32]), u = rbf(gu[2 * 32] + gu[6 * 32]);
            const float sg = rbf(g * __builtin_amdgcn_rcpf(1.f + __builtin_amdgcn_exp2f(-1.4426950408889634f * g)));
            const u32 ab = pkbf(f2{sg * u, 0.f});
            ((LAS u16*)(smem + L_AB + w * 128))[lane] = (u16)ab;
        }
        CFENCE();
        const bf8 bfr = *(const LAS bf8*)(smem + L_AB + w * 128 + (lane & 1) * 64 + tg * 16);
#pragma unroll
        for (int i = 0; i < 5; ++i) {
            if (i == 4 && hiw) break;
            const int ct = (i < 4) ? w + 8 * i : 32 + w4;
            const s16x8 af = __builtin_shufflevector(fd[2 * i], fd[2 * i + 1], 0, 1, 2, 3, 4, 5, 6, 7);
            const f4 d = __builtin_amdgcn_mfma_f32_16x16x32_bf16(__builtin_bit_cast(bf8, af), bfr, f4{0.f, 0.f, 0.f, 0.f}, 0, 0, 0);
            if ((lane & 15) < 2) {
                u32x4 g = {tagd(d.x, g0), tagd(d.y, g1), tagd(d.z, g2), tagd(d.w, g3)};
                __builtin_amdgcn_raw_buffer_store_b128(g, rs, WS_PB + (((lane & 1) * 36 + ct) * 144 + s) * 64 + tg * 16, 0, 16);
            }
        }
        STAMP(11);
    }
    if (!hiw) {
        if (lane < 48) {
#pragma unroll
            for (int hi = 0; hi < 2; ++hi) {
                const u32 row = b + 256 * (w4 + 4 * hi);
                if (row != row1 && row != row2) {
                    u16* o = out + (size_t)row * 576 + lane;
#pragma unroll
                    for (int i = 0; i < 12; ++i) o[48 * i] = (u16)(hi ? po[i] >> 16 : po[i]);
                }
            }
        }
    } else if (fin && w == 5) {
        const int ch = b - 8, tok = ch / 36, ct = ch % 36;
        const u32 row = tok ? row2 : row1, K = tok ? K2 : K1;
        const int c0 = ct * 16, ii = c0 / 48;
        const u32 go = (2 * (c0 % 48 + (lane & 15)) + (ii >= 6 ? 1 : 0)) * 16;
        const int e = ii % 6;
        u32x4 gl, gh;
        for (int spin = 0; spin < SPIN_CAP; ++spin) {
            gl = __builtin_amdgcn_raw_buffer_load_b128(rs, WS_ROWL + row * 1536 + go, 0, 16);
            gh = __builtin_amdgcn_raw_buffer_load_b128(rs, WS_ROWH + tok * 1536 + go, 0, 16);
            if (__builtin_amdgcn_ballot_w64(((gl.w ^ tagA) | (gh.w ^ tagA)) != 0) == 0) break;
            __builtin_amdgcn_s_sleep(2); CFENCE();
        }
        const u32 dl = e < 2 ? gl.x : e < 4 ? gl.y : gl.z, dh = e < 2 ? gh.x : e < 4 ? gh.y : gh.z;
        const float lv = (e & 1) ? bfh(dl) : bfl(dl), hv = (e & 1) ? bfh(dh) : bfl(dh);
        const u32 kb = K >> 16;
        const float rv = bfl((kb & 0x8000u) ? (kb & 0x7FFFu) : (kb ^ 0xFFFFu));
        const float rsel = rbf(__builtin_amdgcn_rcpf(1.f + __builtin_amdgcn_exp2f(-1.4426950408889634f * rv)));
        u32x4 g[9];
        const u32 o = WS_PB + ch * 9216 + lane * 16;
        u32 pend = 511u;
        for (int spin = 0; spin < SPIN_CAP; ++spin) {
#pragma unroll
            for (int i = 0; i < 9; ++i) if (pend >> i & 1) g[i] = __builtin_amdgcn_raw_buffer_load_b128(rs, o, i * 1024, 16);
#pragma unroll
            for (int i = 0; i < 9; ++i) if (pend >> i & 1) {
                u32 bad = ((g[i].x ^ t0) | (g[i].y ^ t1) | (g[i].z ^ t2) | (g[i].w ^ t3)) & 1023u;
                if (__builtin_amdgcn_ballot_w64(bad != 0) == 0) pend &= ~(1u << i);
            }
            if (!pend) break;
            __builtin_amdgcn_s_sleep(2); CFENCE();
        }
        STAMP(12);
        f4 a = {0.f, 0.f, 0.f, 0.f};
#pragma unroll
        for (int i = 0; i < 9; ++i) a += __builtin_bit_cast(f4, g[i]);
        float r[4] = {a.x, a.y, a.z, a.w};
#pragma unroll
        for (int i = 0; i < 4; ++i) {
            r[i] = dppa<0x124, 0xF>(r[i]);
            r[i] = dppa<0x128, 0xF>(r[i]);
            r[i] += __shfl_xor(r[i], 16); r[i] += __shfl_xor(r[i], 32);
        }
        const int src = (lane & 15) >> 2;
        float y = 0.f;
#pragma unroll
        for (int i = 0; i < 4; ++i) { float t = __shfl(r[i], src); if ((lane & 3) == i) y = t; }
        const float upd = rbf(rbf(y) * rsel);
        const u32 ob = pkbf(f2{hv + rbf(lv + upd), 0.f});
        if (lane < 16) out[(size_t)row * 576 + c0 + lane] = (u16)ob;
        STAMP(13);
    }
    const u64 tmin = *(const LAS u64*)(smem + L_TOP + 8);
    while (__builtin_amdgcn_s_memrealtime() < tmin + PAD_US * 100) __builtin_amdgcn_s_sleep(4);
    STAMP(14);
}

extern "C" void kernel_launch(void* const* d_in, const int* in_sizes, int n_in, void* d_out, int out_size, void* d_ws, size_t ws_size,
                              hipStream_t stream) {
    (void)in_sizes; (void)n_in; (void)out_size; (void)ws_size;
    (void)hipFuncSetAttribute((const void*)fused_kernel, hipFuncAttributeMaxDynamicSharedMemorySize, L_TOTAL);
    fused_kernel<<<256, 512, L_TOTAL, stream>>>((const u16*)d_in[0], (const u16*)d_in[1], (const u16*)d_in[2], (const u16*)d_in[4],
                                               (const u16*)d_in[5], (const u16*)d_in[6], (const u16*)d_in[7], (u16*)d_out, (char*)d_ws);
}
```

```cpp
#include <hip/hip_runtime.h>
#include <stdint.h>
#include <type_traits>

typedef unsigned short u16;
typedef unsigned int u32;
typedef unsigned long long u64;
typedef float f2 __attribute__((ext_vector_type(2)));
typedef float f4 __attribute__((ext_vector_type(4)));
typedef u32 u32x2 __attribute__((ext_vector_type(2)));
typedef u32 u32x4 __attribute__((ext_vector_type(4)));
typedef __bf16 bf2 __attribute__((ext_vector_type(2)));
typedef __bf16 bf8 __attribute__((ext_vector_type(8)));
typedef short s16x4 __attribute__((ext_vector_type(4)));
typedef short s16x8 __attribute__((ext_vector_type(8)));
#define DEV __device__ __forceinline__
#define LAS __attribute__((address_space(3)))

#define SPIN_CAP 400000

#define WS_EPOCH   0u
#define WS_ROWH    4096u
#define WS_CAND    8192u
#define WS_S0T     40960u
#define WS_GU      131072u
#define WS_PB      327680u
#define WS_ROWL    (1u << 20)
#define E1_GRP     32896u
#define E1_CLS     (18u * E1_GRP)
#define E2_RS      37120u
#define WS_E1      (2u << 20)
#define WS_E2      (7u << 20)
#define WS_BYTES   (12u << 20)
#define L_Z        0u
#define L_TA       21504u
#define L_WST      41024u
#define L_XS       (L_WST)
#define L_AB       (L_WST + 3584u)
#define L_KEYS     49216u
#define L_TOP      (L_KEYS + 32u)
#define L_FLAG     (L_KEYS + 64u)
#define L_BIG      49344u
#define L_TB       (L_BIG + 16u)
#define L_WSTB     (L_BIG + 19584u)
#define L_S0       (L_BIG + 32768u)
#define L_WH       (L_BIG + 110592u)
#define L_JUNK     (L_WH + 2688u)
#define L_TOTAL    (L_JUNK + 1024u)
#define L_TW       (L_WST)

DEV f2 pfma(f2 a, f2 b, f2 c) { return __builtin_elementwise_fma(a, b, c); }
#define PM f2{1.f, -1.f}
#define MP f2{-1.f, 1.f}
DEV f2 add_mi(f2 a, f2 b) { return pfma(b.yx, PM, a); }
DEV f2 add_pi(f2 a, f2 b) { return pfma(b.yx, MP, a); }
DEV f2 nadd_mi(f2 a, f2 b) { return pfma(b.yx, PM, -a); }
DEV f2 nadd_pi(f2 a, f2 b) { return pfma(b.yx, MP, -a); }
DEV f2 add_conj(f2 a, f2 b) { return pfma(b, PM, a); }
DEV f2 sub_conj(f2 a, f2 b) { return pfma(b, MP, a); }
DEV f2 odd_sep(f2 a, f2 b) { return pfma(a.yx, PM, b.yx); }
DEV f2 sumdiff(f2 a) { return pfma(a.yy, PM, a.xx); }
struct Tw { f2 x; f2 n; };
DEV Tw mktw(f2 w) { return Tw{w.xx, f2{-w.y, w.y}}; }
DEV f2 cmul(f2 a, Tw w) { return pfma(a, w.x, a.yx * w.n); }
DEV f2 cmulc(f2 a, Tw w) { return pfma(a, w.x, -(a.yx * w.n)); }
template<int N> struct TwA {
    f2 xp[(N + 1) / 2]; f2 n[N];
    DEV Tw operator[](int i) const { return Tw{(i & 1) ? xp[i / 2].yy : xp[i / 2].xx, n[i]}; }
    DEV void set(int i, float x, f2 nn) { if (i & 1) xp[i / 2].y = x; else xp[i / 2].x = x; n[i] = nn; }
    DEV void pin() {
#pragma unroll
        for (int i = 0; i < (N + 1) / 2; ++i) asm volatile("" : "+v"(xp[i]));
#pragma unroll
        for (int i = 1; i < N; ++i) asm volatile("" : "+v"(n[i]));
    }
};
template<int DIR> DEV f2 addr_(f2 a, f2 b) { return DIR > 0 ? add_mi(a, b) : add_pi(a, b); }
template<int DIR> DEV f2 subr_(f2 a, f2 b) { return DIR > 0 ? add_pi(a, b) : add_mi(a, b); }
template<int DIR> DEV f2 naddr_(f2 a, f2 b) { return DIR > 0 ? nadd_mi(a, b) : nadd_pi(a, b); }

#define C_H 0.70710678118654752440f
#define C_K 0.86602540378443864676f
template<int DIR> DEV void r4(f2& a, f2& b, f2& c, f2& d) {
    f2 s0 = a + c, s1 = a - c, s2 = b + d, s3 = b - d;
    a = s0 + s2; c = s0 - s2; b = addr_<DIR>(s1, s3); d = subr_<DIR>(s1, s3);
}
template<int DIR> DEV void r3(f2& a, f2& b, f2& c) {
    f2 t = b + c, d = b - c;
    f2 m = pfma(t, f2{-0.5f, -0.5f}, a);
    a = a + t;
    const f2 kk = DIR > 0 ? f2{C_K, -C_K} : f2{-C_K, C_K};
    b = pfma(d.yx, kk, m);
    c = pfma(d.yx, -kk, m);
}
template<int DIR> DEV void r4odd(f2& a, f2& b, f2& c, f2& d) {
    f2 o0 = addr_<DIR>(a, c), o2 = subr_<DIR>(a, c);
    f2 p = addr_<DIR>(b, d), m = subr_<DIR>(b, d);
    f2 tp = addr_<DIR>(p, p), tm = naddr_<DIR>(m, m);
    const f2 hh = f2{C_H, C_H};
    a = pfma(tp, hh, o0); c = pfma(tp, -hh, o0); b = pfma(tm, hh, o2); d = pfma(tm, -hh, o2);
}
template<int DIR> DEV void r8(f2* v) {
    f2 a0 = v[0] + v[4], a4 = v[0] - v[4], a1 = v[1] + v[5], a5 = v[1] - v[5];
    f2 a2 = v[2] + v[6], a6 = v[2] - v[6], a3 = v[3] + v[7], a7 = v[3] - v[7];
    r4<DIR>(a0, a1, a2, a3);
    r4odd<DIR>(a4, a5, a6, a7);
    v[0] = a0; v[2] = a1; v[4] = a2; v[6] = a3; v[1] = a4; v[3] = a5; v[5] = a6; v[7] = a7;
}
template<int DIR> DEV void r6(f2* v) {
    r3<DIR>(v[0], v[2], v[4]); r3<DIR>(v[3], v[5], v[1]);
    const f2 o0 = v[0] + v[3], o3 = v[0] - v[3], o4 = v[2] + v[5], o1 = v[2] - v[5], o2 = v[4] + v[1], o5 = v[4] - v[1];
    v[0] = o0; v[1] = o1; v[2] = o2; v[3] = o3; v[4] = o4; v[5] = o5;
}
template<int N> DEV void twfill(TwA<N>& t, int base, float invN) {
    const float st = (float)base * invN;
    t.xp[0].x = 1.f;
#pragma unroll
    for (int i = 1; i < N; ++i) { const float x = st * (float)i; const float sn = __builtin_amdgcn_sinf(x); t.set(i, __builtin_amdgcn_cosf(x), f2{sn, -sn}); }
    t.pin();
}
#define CFENCE() asm volatile("" ::: "memory")
DEV f2 ldz(const LAS f2* p) { return *(const volatile LAS f2*)p; }
DEV void stz(LAS f2* p, f2 v) { *p = v; }
DEV void fft288_fwd(f2* v, const TwA<6>& tw1, const TwA<6>& tw2, LAS f2* Z, int lane) {
    r6<1>(v);
#pragma unroll
    for (int i = 1; i < 6; ++i) v[i] = cmul(v[i], tw1[i]);
#pragma unroll
    for (int i = 0; i < 6; ++i) stz(Z + i * 56 + lane, v[i]);
    CFENCE();
#pragma unroll
    for (int i = 0; i < 6; ++i) v[i] = ldz(Z + (lane >> 3) * 56 + 8 * i + (lane & 7));
    r6<1>(v);
#pragma unroll
    for (int i = 1; i < 6; ++i) v[i] = cmul(v[i], tw2[i]);
    CFENCE();
#pragma unroll
    for (int i = 0; i < 6; ++i) stz(Z + (lane & 7) * 38 + (lane >> 3) + 6 * i, v[i]);
    CFENCE();
    if (lane < 36) {
#pragma unroll
        for (int c = 0; c < 8; ++c) v[c] = ldz(Z + c * 38 + lane);
        r8<1>(v);
        const f2 P = sumdiff(v[0]), Q = v[4] * PM;
        if (lane == 0) { v[0] = P; v[4] = Q; }
        CFENCE();
        Z[lane == 0 ? 288 : lane + 144] = v[4];
#pragma unroll
        for (int c = 0; c < 8; ++c) if (c != 4) stz(Z + lane + 36 * c, v[c]);
    }
}
DEV float bfl(u32 u) { return __uint_as_float(u << 16); }
DEV float bfh(u32 u) { return __uint_as_float(u & 0xFFFF0000u); }
DEV u32 pkbf(f2 v) { bf2 b = __builtin_convertvector(v, bf2); return __builtin_bit_cast(u32, b); }
DEV f2 unbf(u32 u) { return f2{bfl(u), bfh(u)}; }
DEV f2 rbf2(f2 v) { return unbf(pkbf(v)); }
DEV float rbf(float v) { return bfl(pkbf(f2{v, v})); }

template<int CTRL> DEV float dppa(float v) { return v + __int_as_float(__builtin_amdgcn_update_dpp(0, __float_as_int(v), CTRL, 0xF, 0xF, true)); }
DEV float sw32(float a, float b) { auto r = __builtin_amdgcn_permlane32_swap(__float_as_uint(a), __float_as_uint(b), false, false); return __uint_as_float(r[0]) + __uint_as_float(r[1]); }
DEV float sw16(float a, float b) { auto r = __builtin_amdgcn_permlane16_swap(__float_as_uint(a), __float_as_uint(b), false, false); return __uint_as_float(r[0]) + __uint_as_float(r[1]); }
DEV float rowsum(float s) { s = dppa<0x121>(s); s = dppa<0x122>(s); s = dppa<0x124>(s); return dppa<0x128>(s); }
template<int CTRL, int RM> DEV u32 dppm(u32 v) { u32 o = (u32)__builtin_amdgcn_update_dpp(0, (int)v, CTRL, RM, 0xF, false); return v > o ? v : o; }
DEV u32 wmax(u32 v) {
    v = dppm<0x111, 0xF>(v); v = dppm<0x112, 0xF>(v); v = dppm<0x114, 0xF>(v); v = dppm<0x118, 0xF>(v);
    v = dppm<0x142, 0xA>(v); v = dppm<0x143, 0xC>(v);
    return (u32)__builtin_amdgcn_readlane((int)v, 63);
}
DEV float rdl(float v, int l) { return __int_as_float(__builtin_amdgcn_readlane(__float_as_int(v), l)); }

DEV void bar() { asm volatile("s_waitcnt lgkmcnt(0)\n\ts_barrier" ::: "memory"); }
DEV void vm0() { asm volatile("s_waitcnt vmcnt(0)" ::: "memory"); }
DEV void glds16s(u32 voff, const void* sbase, u32 lds_dst) {
    u32 keep;
    asm volatile("s_mov_b32 %0, m0\n\ts_mov_b32 m0, %3\n\ts_nop 0\n\tglobal_load_lds_dwordx4 %1, %2\n\ts_mov_b32 m0, %0" : "=&s"(keep) : "v"(voff), "s"(sbase), "s"(lds_dst) : "memory");
}
DEV void glds4(u32 voff, const void* sbase, u32 lds_dst) {
    u32 keep;
    asm volatile("s_mov_b32 %0, m0\n\ts_mov_b32 m0, %3\n\ts_nop 0\n\tglobal_load_lds_dword %1, %2\n\ts_mov_b32 m0, %0" : "=&s"(keep) : "v"(voff), "s"(sbase), "s"(lds_dst) : "memory");
}
DEV void glds2(u32 voff, const void* a, const void* b2, u32 dst) {
    u32 keep;
    asm volatile("s_mov_b32 %0, m0\n\ts_mov_b32 m0, %4\n\ts_nop 0\n\tglobal_load_lds_dwordx4 %1, %2 nt\n\t"
                 "s_add_u32 m0, m0, 1024\n\ts_nop 0\n\tglobal_load_lds_dwordx4 %1, %3 nt\n\ts_mov_b32 m0, %0"
                 : "=&s"(keep) : "v"(voff), "s"(a), "s"(b2), "s"(dst) : "memory", "scc");
}
DEV u64 mixtag(u64 e) {
    u64 z = (e + 1) * 0x9E3779B97F4A7C15ull;
    z = (z ^ (z >> 30)) * 0xBF58476D1CE4E5B9ull; z = (z ^ (z >> 27)) * 0x94D049BB133111EBull; return z ^ (z >> 31);
}
DEV u32 tagd(float v, u32 t) { return (__float_as_uint(v) & 0xFFFFFC00u) ^ t; }

#define PIN1(x) asm volatile("" : "+v"(x))
#define SPIN_LDS(cond) do { _Pragma("unroll 1") for (int spin_ = 0; spin_ < SPIN_CAP; ++spin_) { if (cond) break; __builtin_amdgcn_s_sleep(1); } CFENCE(); } while (0)
#define RFL(x) ((u32)__builtin_amdgcn_readfirstlane(x))
#define BALLOT(c) __builtin_amdgcn_ballot_w64(c)
#define LD128(off, so) __builtin_amdgcn_raw_buffer_load_b128(rs, (off), (so), 16)
#define LD64(off, so) __builtin_amdgcn_raw_buffer_load_b64(rs, (off), (so), 16)
#define ST128(v, off) __builtin_amdgcn_raw_buffer_store_b128((v), rs, (off), 0, 16)
#define ST64(v, off, so) __builtin_amdgcn_raw_buffer_store_b64((v), rs, (off), (so), 16)
#define CODE_BYTES 29696u
#define WDLY 4
#define S1 3
#define S2 4
#define S3 7
#define GU_DLY 40
#define NM_DLY 44
#define Q_WAIT 20
#define FIN_WAIT 12
DEV f2 asf2(u32 a, u32 b) { return f2{__uint_as_float(a), __uint_as_float(b)}; }
DEV u32 e1off(int slot, int j) { return (slot >> 4) * E1_GRP + j * 128 + (slot & 15) * 8; }
DEV u32 e2off(int slot, int j) {
    const u32 hf = (j >> 3) & 1, base = ((j & 7) | ((j >> 4) << 3)) * E2_RS;
    return base + (slot < 224 ? (slot >> 3) * 128 + hf * 64 + (slot & 7) * 8 : 3584 + hf * 512 + (slot - 224) * 8);
}
#define ZST(off, so) __builtin_amdgcn_raw_buffer_store_b128((u32x4{0u, 0u, 0u, 0u}), rs, (off) + (so), 0, 16)

__global__ void __launch_bounds__(768, 3) fused_kernel(const u16* __restrict__ x, const u16* __restrict__ awr, const u16* __restrict__ awi,
                                                       const u16* __restrict__ bwr, const u16* __restrict__ wg, const u16* __restrict__ wu,
                                                       const u16* __restrict__ wd, u16* out, char* ws, const u64* __restrict__ ep) {
    extern __shared__ __attribute__((aligned(16))) char smem[];
    const int b = blockIdx.x;
    const int lane = threadIdx.x & 63;
    const int w = __builtin_amdgcn_readfirstlane(threadIdx.x >> 6);
    if (b < 16 && w < 4) {
        const char* pc0; asm volatile("s_getpc_b64 %0" : "=s"(pc0));
        const u32 co = threadIdx.x * 128u;
        glds4(co < CODE_BYTES ? co : CODE_BYTES, pc0, (u32)(uintptr_t)smem + L_JUNK);
    }
    asm volatile("" :: "s"(x), "s"(awr), "s"(awi), "s"(bwr), "s"(wg), "s"(wu), "s"(wd), "s"(out), "s"(ws), "s"(ep));
    u32 xr[6];
    const int l48 = lane < 48 ? lane : 47;
    const u32 row = b + 256 * (w & 7);
    {
        const u32* xa = (const u32*)(x + (size_t)row * 576) + l48;
#pragma unroll
        for (int i = 0; i < 6; ++i) xr[i] = xa[48 * i];
        CFENCE();
    }
    const u64 epoch = *(const __attribute__((address_space(4))) u64*)(uintptr_t)ep;
    const int w4 = w & 3;
    const int cls = b & 7;
    const int rsym = cls <= 4 ? cls : cls - 8;
    const bool dbl = b >= 224;
    const bool moe = (b >= 80) && (b < 224);
    const bool qb = (b >= 8) && (b < 80);
    const int k0 = dbl ? 224 + 16 * ((b >> 3) - 28) : (b >> 3) * 8;
    const u32 lbase = (u32)(uintptr_t)smem;
    const __amdgpu_buffer_rsrc_t rs = __builtin_amdgcn_make_buffer_rsrc(ws, 0, WS_BYTES, 0x00020000);
    volatile LAS u32* const flag = (volatile LAS u32*)(smem + L_FLAG);
    if (w == 0 && lane < 28) ((volatile LAS u32*)(smem + L_KEYS))[lane] = 0;
    {
        if (w == 8 || (w == 9 && dbl)) {
            const int c0 = k0 + (w == 9 ? 8 : 0) + (k0 >= 144 ? 1 : 0);
            const u32 dst = lbase + (w == 9 ? L_WSTB : L_WST);
#pragma unroll
            for (int i = 0; i < 4; ++i) {
                const int ir = (i + (b >> 3)) & 3;
                const u32 eo = (((8 * (lane + 64 * ir) + rsym) & 2047) * 289 + c0) * 2;
                glds16s(eo, awr, dst + ir * 1024); glds16s(eo, awi, dst + 4096 + ir * 1024);
            }
        }
    }
    u32 t0, t1, t2, t3, tagA, tagB;
    auto tags = [&]() {
        u64 e = epoch; asm volatile("" : "+s"(e));
        const u64 T = mixtag(e);
        t0 = ((u32)T & 1023u) | 512u; t1 = (u32)(T >> 10) & 1023u; t2 = (u32)(T >> 20) & 1023u; t3 = (u32)(T >> 30) & 1023u;
        tagA = (u32)T | 1u; tagB = (u32)(T >> 32);
    };
    if (w == 10) {
        const int k1 = (8 * b + (lane & 7)) * 289;
        const u32 r0 = awr[k1], q0 = awi[k1], r1 = awr[k1 + 288], q1 = awi[k1 + 288], r2 = awr[k1 + 144], q2 = awi[k1 + 144];
        tags();
        u32x4 g = u32x4{r0 | (q0 << 16), r1 | (q1 << 16), r2 | (q2 << 16), tagA};
        PIN1(g);
        if (lane < 8) ST128(g, WS_S0T + lane * 4096 + b * 16);
    }
    const int tq = (lane & 15) >> 2, tp = lane & 3, tg = lane >> 4;

    if (w >= 8) {
        if (w != 10) __builtin_amdgcn_s_setprio(3);
        tags();
        int wn = 0;
        const int lw = w == 11 ? 2 : w4;
        const u32 voff = qb ? lane * 16u : (u32)(lane >> 3) * 9216u + (lane & 7) * 16u, pstep = qb ? 1024u : 73728u;
        const char* const wsrc = qb ? (const char*)wd + (b - 8) * 73728 : b >= 152 ? (const char*)wu + (b - 152) * 128 : (const char*)wg + (b - 80) * 128;
        auto bgload = [&](u32 st, int upto) {
            if (!(moe || qb) || w == 10) return;
            if (st) SPIN_LDS(RFL(*flag) >= st);
#pragma unroll 1
            for (; wn < upto; ++wn) {
                const u32 pc = (wn * 3 + lw) * 2;
                glds2(voff, wsrc + pc * pstep, wsrc + (pc + 1) * pstep, lbase + L_BIG + pc * 1024);
            }
        };
        if (w == 10) {
            f2 v[8];
#pragma unroll
            for (int i = 0; i < 6; ++i) v[i] = unbf(((const u32*)bwr)[l48 + 48 * i]);
            SPIN_LDS(RFL(flag[2]) == 1);
            {
                const u32 l8 = lane >> 3;
#pragma unroll 1
                for (int n = 0; n < 3; ++n) ZST(WS_ROWL + b * 3072 + lane * 16, n * 1024);
                if (moe) ZST(WS_GU + (b - 80) * 1024 + lane * 16, 0);
                if (qb) {
#pragma unroll 1
                    for (int n = 0; n < 5; ++n) if (n * 8 + l8 < 36) ZST(WS_PB + (l8 * 72 + (b - 8)) * 128 + (lane & 7) * 16, n * 8 * 9216);
                }
            }
            bar();
            TwA<6> tw1, tw2;
            twfill(tw1, l48, 1.f / 288.f); twfill(tw2, l48 & 7, 1.f / 48.f);
            if (lane < 48) fft288_fwd(v, tw1, tw2, (LAS f2*)(smem + L_WH), lane);
            bar();
            if (lane < 48) {
                LAS f4* t = (LAS f4*)(smem + L_TW) + lane;
                t[0 * 48] = f4{tw1.xp[0].x, tw1.xp[0].y, tw1.xp[1].x, tw1.xp[1].y}; t[1 * 48] = f4{tw1.xp[2].x, tw1.xp[2].y, tw1.n[1].x, tw1.n[1].y};
                t[2 * 48] = f4{tw1.n[2].x, tw1.n[2].y, tw1.n[3].x, tw1.n[3].y}; t[3 * 48] = f4{tw1.n[4].x, tw1.n[4].y, tw1.n[5].x, tw1.n[5].y};
                t[4 * 48] = f4{tw2.xp[0].x, tw2.xp[0].y, tw2.xp[1].x, tw2.xp[1].y}; t[5 * 48] = f4{tw2.xp[2].x, tw2.xp[2].y, tw2.n[1].x, tw2.n[1].y};
                t[6 * 48] = f4{tw2.n[2].x, tw2.n[2].y, tw2.n[3].x, tw2.n[3].y}; t[7 * 48] = f4{tw2.n[4].x, tw2.n[4].y, tw2.n[5].x, tw2.n[5].y};
            }
        } else if (w == 8 || (w == 9 && dbl)) {
            bar();
            vm0();
            flag[w == 9 ? 3 : 1] = 1;
        } else {
            if (moe || qb) { SPIN_LDS(RFL(flag[2]) == 1); __builtin_amdgcn_s_sleep(WDLY); }
            bgload(0, S1);
            bar();
            if (w == 11 && (b < 8 || b >= 248)) {
                u32x4 ga[4], gb[4];
#pragma unroll 1
                for (int spin = 0; spin < SPIN_CAP; ++spin) {
                    u32 bad = 0;
#pragma unroll
                    for (int i = 0; i < 4; ++i) {
                        const int k1 = (8 * (lane + 64 * i) + rsym) & 2047, km = (2048 - k1) & 2047;
                        ga[i] = LD128(WS_S0T + (k1 & 7) * 4096 + (k1 >> 3) * 16, 0);
                        gb[i] = LD128(WS_S0T + (km & 7) * 4096 + (km >> 3) * 16, 0);
                    }
#pragma unroll
                    for (int i = 0; i < 4; ++i) bad |= (ga[i].w ^ tagA) | (gb[i].w ^ tagA);
                    if (BALLOT(bad != 0) == 0) break;
                    __builtin_amdgcn_s_sleep(2); CFENCE();
                }
#pragma unroll
                for (int i = 0; i < 4; ++i) {
                    const f2 h0 = add_conj(unbf(ga[i].x), unbf(gb[i].x)), hn = add_conj(unbf(ga[i].y), unbf(gb[i].y));
                    const float sc = 0.5f / (2048.f * 576.f);
                    f2 al, be = f2{0.f, 0.f};
                    if (b >= 248) al = unbf(ga[i].z) * sc;
                    else if ((cls & 3) == 0) { al = (h0 + hn) * (sc * 0.125f); be = (h0 - hn) * (sc * 0.125f); }
                    else al = (cls < 4 ? h0 : hn) * (sc * 0.125f);
                    *(LAS f4*)(smem + L_S0 + (lane + 64 * i) * 16) = f4{al.x, al.y, be.x, be.y};
                }
                asm volatile("s_waitcnt lgkmcnt(0)" ::: "memory");
                flag[4] = 1;
            }
        }
        if (w != 10) bar();
        bgload(0, S2);
        if (w == 10) vm0();
        bar();
        bgload(6, S3);
        bar();
        if ((moe || qb) && w != 10) {
            bgload(0, 12);
            vm0();
            flag[w4 + 8] = 1;
        }
        __builtin_amdgcn_s_setprio(3);
        if (w == 10) {
            u32 kk = 0;
#pragma unroll 1
            for (int spin = 0; spin < SPIN_CAP; ++spin) { kk = ((volatile LAS u32*)(smem + L_KEYS))[lane & 7]; if (BALLOT(kk == 0) == 0) break; }
            u32 m1 = dppm<0x124, 0xF>(dppm<0x122, 0xF>(dppm<0x121, 0xF>(kk)));
            u32 m2 = dppm<0x124, 0xF>(dppm<0x122, 0xF>(dppm<0x121, 0xF>(kk == m1 ? 0u : kk)));
            if (lane < 8) ST128((u32x4{m1, m2, tagA, tagB}), WS_CAND + lane * 4096 + b * 16);
            const u32 own1 = m1, own2 = m2;
            if (moe) __builtin_amdgcn_s_sleep(GU_DLY); else __builtin_amdgcn_s_sleep(NM_DLY);
            u32x4 g[4];
            u32 oc = WS_CAND + cls * 4096 + lane * 16;
#pragma unroll 1
            for (int spin = 0; spin < SPIN_CAP; ++spin) {
                CFENCE();
#pragma unroll
                for (int i = 0; i < 4; ++i) g[i] = LD128(oc, i * 1024);
                u32 bad = 0;
#pragma unroll
                for (int i = 0; i < 4; ++i) bad |= (lane + 64 * i == b) ? 0u : ((g[i].z ^ tagA) | (g[i].w ^ tagB));
                if (BALLOT(bad != 0) == 0) break;
            }
            m1 = 0; m2 = 0;
#pragma unroll
            for (int i = 0; i < 4; ++i) {
                const bool me_ = lane + 64 * i == b;
                const u32 x = me_ ? own1 : g[i].x, y = me_ ? own2 : g[i].y;
                u32 lo = x < m1 ? x : m1; m1 = x > m1 ? x : m1;
                lo = y > lo ? y : lo; m2 = lo > m2 ? lo : m2;
            }
            const u32 K1 = wmax(m1);
            const u32 K2 = wmax(m1 == K1 ? m2 : m1);
            if (lane == 0) *(LAS u32x2*)(smem + L_TOP) = u32x2{K1, K2};
            if (moe) {
                u32x4 r[3];
                const u32 o1 = WS_ROWL + (~K1 & 255u) * 3072 + lane * 16, o2 = WS_ROWL + (~K2 & 255u) * 3072 + (((K1 ^ K2) & 255u) ? 0u : 1536u) + lane * 16;
#pragma unroll 1
                for (int spin = 0; spin < SPIN_CAP; ++spin) {
                    CFENCE();
                    r[0] = LD128(o1, 0); r[1] = LD128(lane < 32 ? o1 + 1024 : o2 - 512, 0); r[2] = LD128(o2 + 512, 0);
                    if (BALLOT(((r[0].w ^ tagA) | (r[1].w ^ tagA) | (r[2].w ^ tagA)) != 0) == 0) break;
                }
#pragma unroll
                for (int i = 0; i < 3; ++i) {
                    const int n = lane + 64 * i, m = n >= 96 ? n - 96 : n;
                    LAS u32* d = (LAS u32*)(smem + L_XS) + (n >= 96 ? 288 : 0) + (m >> 1) + 144 * (m & 1);
                    d[0] = r[i].x; d[48] = r[i].y; d[96] = r[i].z;
                }
            }
        }
        bar();
        if (qb && w == 10) {
            __builtin_amdgcn_s_sleep(Q_WAIT);
            u32x4 gg, gu;
            const u32 og = WS_GU + (b - 8) * 1024 + (lane & 31) * 16;
#pragma unroll 1
            for (int spin = 0; spin < SPIN_CAP; ++spin) {
                CFENCE();
                gg = LD128(og, 0); gu = LD128(og, 72 * 1024);
                if (BALLOT(((gg.z ^ tagA) | (gg.w ^ tagB) | (gu.z ^ tagA) | (gu.w ^ tagB)) != 0) == 0) break;
            }
            ((LAS u32x2*)(smem + L_AB))[lane & 31] = u32x2{pkbf(unbf(gg.x) * unbf(gu.x)), pkbf(unbf(gg.y) * unbf(gu.y))};
        }
        if (qb) bar();
        if (w == 9 && qb) {
            const u32x2 top = *(const LAS u32x2*)(smem + L_TOP);
            const int ch = b - 8, tok = ch >= 36, ct = ch - 36 * tok;
            const u32 K = RFL(tok ? top.y : top.x);
            const u32 row = (0xFFFFu - (K & 0xFFFFu)) & 2047u;
            const int el = tg == 1 ? 2 : tg == 2 ? 1 : tg, L = tp * 4 + el;
            const int n = ct * 8 + (L >> 1), ii = (ct * 8) / 48;
            const u32 go = (2 * (n % 48) + (ii >= 3 ? 1 : 0)) * 16;
            const int e = ii % 3;
            const u32 kb = K >> 16;
            const float rv = bfl((kb & 0x8000u) ? (kb & 0x7FFFu) : (kb ^ 0xFFFFu));
            const float rsel = rbf(__builtin_amdgcn_rcpf(1.f + __builtin_amdgcn_exp2f(-1.4426950408889634f * rv)));
            u32x4 g[5], gl, gh;
            const u32 o = WS_PB + ct * 9216 + (lane >> 2) * 128 + tok * 64 + (lane & 3) * 16;
            const u32 ol = WS_ROWL + (row & 255u) * 3072 + ((tok && ((RFL(top.x) ^ K) & 255u) == 0) ? 1536u : 0u) + go, oh = WS_ROWH + tok * 1536 + go;
            __builtin_amdgcn_s_sleep(FIN_WAIT);
#pragma unroll 1
            for (int spin = 0; spin < SPIN_CAP; ++spin) {
                CFENCE();
#pragma unroll
                for (int i = 0; i < 4; ++i) g[i] = LD128(o, i * 2048);
                g[4] = LD128(lane < 32 ? o + 8192 : o, 0);
                gl = LD128(ol, 0); gh = LD128(oh, 0);
                u32 bad = 0;
#pragma unroll
                for (int i = 0; i < 5; ++i) { g[i].x ^= t0; g[i].y ^= t1; g[i].z ^= t2; g[i].w ^= t3; bad |= g[i].x | g[i].y | g[i].z | g[i].w; }
                if (BALLOT(((bad & 1023u) | (gl.w ^ tagA) | (gh.w ^ tagA)) != 0) == 0) break;
            }
            const u32 dl = e == 0 ? gl.x : e == 1 ? gl.y : gl.z, dh = e == 0 ? gh.x : e == 1 ? gh.y : gh.z;
            const float lv = (L & 1) ? bfh(dl) : bfl(dl), hv = (L & 1) ? bfh(dh) : bfl(dh);
            if (lane >= 32) g[4] = u32x4{0u, 0u, 0u, 0u};
            const f4 a = (__builtin_bit_cast(f4, g[0]) + __builtin_bit_cast(f4, g[1])) + (__builtin_bit_cast(f4, g[2]) + __builtin_bit_cast(f4, g[3])) + __builtin_bit_cast(f4, g[4]);
            float t = sw16(sw32(a.x, a.y), sw32(a.z, a.w));
            t = dppa<0x124>(t); t = dppa<0x128>(t);
            const float upd = rbf(rbf(t) * rsel);
            const u32 ob = pkbf(f2{hv + rbf(lv + upd), 0.f});
            if (tq == 0) out[(size_t)row * 576 + ct * 16 + L] = (u16)ob;
        } else if (b == 0 && w == 11) {
            if (lane == 0) __hip_atomic_store((u64*)(ws + WS_EPOCH), epoch + 1, __ATOMIC_RELAXED, __HIP_MEMORY_SCOPE_AGENT);
        }
    } else {
        __builtin_amdgcn_s_setprio(2);
        auto announce = [&](u32 ph) { if (w == 0) *flag = ph; };
        LAS f2* const Zw = (LAS f2*)(smem + L_Z) + w * 336;
        TwA<6> tw1, tw2;
        twfill(tw1, l48, 1.f / 288.f); twfill(tw2, l48 & 7, 1.f / 48.f);
        const int h = w >= 3;
        int kb0 = (w - 3 * h) * 48; asm volatile("" : "+s"(kb0));
        const int kI = kb0 + l48;
        TwA<4> wc;
        Tw cw[4], u256;
        u32 od, om, o2d, o2m, tdx, tdy, tmx, tmy;
        if (w < 6) {
            const float fk = kI == 0 ? -0.25f : (float)kI * (1.f / 576.f);
            const float fj = (float)b * (1.f / 2048.f);
            const int cl = 2 * (lane & 3) + h;
            const float al = fj * (float)((lane & 4) ? 8 : cl <= 4 ? cl : cl - 8);
            const float uc = __builtin_amdgcn_cosf(al), us = __builtin_amdgcn_sinf(al);
#pragma unroll
            for (int i = 0; i < 4; ++i) {
                const float s = fk + rdl(al, i);
                const float sn = __builtin_amdgcn_sinf(s);
                wc.set(i, __builtin_amdgcn_cosf(s), f2{sn, -sn});
                const float cx = rdl(uc, i), cy = rdl(us, i);
                cw[i] = Tw{f2{cx, cx}, f2{cy, -cy}};
            }
            const float cx = rdl(uc, 4), cy = rdl(us, 4);
            u256 = Tw{f2{cx, cx}, f2{cy, -cy}};
            wc.pin(); PIN1(wc.n[0]);
            tags();
            od = WS_E1 + e1off(kI, b); om = WS_E1 + e1off(287 - kI, b);
            o2d = WS_E2 + e2off(kI, b); o2m = WS_E2 + e2off(287 - kI, b);
            tdx = (lane & 1) ? t2 : t0; tdy = (lane & 1) ? t3 : t1; tmx = (lane & 1) ? t0 : t2; tmy = (lane & 1) ? t1 : t3;
            PIN1(od); PIN1(om); PIN1(o2d); PIN1(o2m); PIN1(tdx); PIN1(tdy); PIN1(tmx); PIN1(tmy);
        } else tags();
#pragma unroll
        for (int i = 0; i < 6; ++i) PIN1(xr[i]);
        if (w == 0) flag[2] = 1;
        float mu1, var1, sd1, dm1;
        {
            f2 v[8], xv[6];
#pragma unroll
            for (int i = 0; i < 6; ++i) xv[i] = unbf(xr[i]);
            const float c1 = rdl(xv[0].x, 0);
#pragma unroll
            for (int i = 0; i < 6; ++i) v[i] = xv[i] - c1;
            const f2 a1 = (v[0] + v[1]) + (v[2] + v[3]) + (v[4] + v[5]);
            const f2 a2 = pfma(v[0], v[0], pfma(v[2], v[2], v[4] * v[4])) + pfma(v[1], v[1], pfma(v[3], v[3], v[5] * v[5]));
            float s1 = lane < 48 ? a1.x + a1.y : 0.f, s2 = lane < 48 ? a2.x + a2.y : 0.f;
            const float e = sw32(s1, s2);
            const float t = rowsum(sw16(e, e));
            s1 = rdl(t, 0); s2 = rdl(t, 32);
            const float md = s1 * (1.f / 576.f);
            const float var = fmaxf(fmaf(-md, md, s2 * (1.f / 576.f)), 0.f);
            const f2 mv = rbf2(f2{c1 + md, var});
            sd1 = rbf(__builtin_amdgcn_sqrtf(rbf(mv.y + 1e-5f)));
            const float rsd = __builtin_amdgcn_rcpf(sd1);
            const float off = (c1 - mv.x) * rsd;
            mu1 = c1 + md; var1 = var; dm1 = (mv.x - c1) - md;
            if (lane < 48) {
#pragma unroll
                for (int i = 0; i < 6; ++i) v[i] = pfma(v[i], f2{rsd, rsd}, f2{off, off});
                fft288_fwd(v, tw1, tw2, Zw, lane);
            }
        }
        bar();
        auto class_fwd = [&](auto HC) {
            constexpr int H = decltype(HC)::value;
            const LAS f2* zp = (const LAS f2*)(smem + L_Z) + kI;
            const LAS f2* zq = (const LAS f2*)(smem + L_Z) + 288 - kI;
            f2 A[4], B[4];
            {
                f2 p[8], q[8];
#pragma unroll
                for (int m = 0; m < 8; ++m) { p[m] = zp[m * 336]; q[m] = zq[m * 336]; }
#pragma unroll
                for (int a = 0; a < 4; ++a) { A[a] = H ? p[a] - p[a + 4] : p[a] + p[a + 4]; B[a] = H ? q[a] - q[a + 4] : q[a] + q[a + 4]; }
            }
            if (H) { r4odd<1>(A[0], A[1], A[2], A[3]); r4odd<1>(B[0], B[1], B[2], B[3]); }
            else { r4<1>(A[0], A[1], A[2], A[3]); r4<1>(B[0], B[1], B[2], B[3]); }
            f2 Dv[4], Mv[4];
#pragma unroll
            for (int i = 0; i < 4; ++i) {
                const int ni = H ? 3 - i : (4 - i) & 3;
                f2 e = add_conj(A[i], B[ni]);
                const f2 t = cmul(sub_conj(A[i], B[ni]), wc[i]);
                if (H || i) e = cmul(e, cw[i]);
                Dv[i] = add_mi(e, t);
                Mv[ni] = add_pi(e, t);
            }
            if (!H) Mv[2] = cmulc(Mv[2], u256);
            if (w == 3 * H) {
                if (H) {
                    const f2 a0 = add_conj(Dv[0], Dv[3]), a3 = odd_sep(Dv[3], Dv[0]), a1 = add_conj(Dv[1], Dv[2]), a2 = odd_sep(Dv[2], Dv[1]);
                    if (lane == 0) { Dv[0] = a0; Dv[1] = a1; Dv[2] = a2; Dv[3] = a3; }
                } else {
                    const f2 a1 = add_conj(Dv[1], Dv[3]), a3 = odd_sep(Dv[3], Dv[1]);
                    if (lane == 0) { Dv[1] = a1; Dv[3] = a3; }
                }
            }
            const u32 tms = tmy | 0x80000000u;
#pragma unroll
            for (int i = 0; i < 4; ++i) {
                ST64((u32x2{tagd(Dv[i].x, tdx), tagd(Dv[i].y, tdy)}), od, (2 * i + H) * E1_CLS);
                ST64((u32x2{tagd(Mv[i].x, tmx), tagd(Mv[i].y, tms)}), om, (2 * i + H) * E1_CLS);
            }
        };
        if (w < 6 && lane < 48) {
            if (h) class_fwd(std::integral_constant<int, 1>{}); else class_fwd(std::integral_constant<int, 0>{});
        }
        const bool hiw = w >= 4;
        const bool p2act = !hiw || dbl;
        LAS f2* const TT = (LAS f2*)(smem + (hiw ? L_TB : L_TA));
        const int mp = lane >> 4, mt = (lane >> 1) & 7, me = lane & 1;
        const int jj = dbl ? (lane >> 3) + 8 * w : ((lane >> 3) | ((lane & 4) << 1)) + 16 * w4, kp = lane & 3, kpx = dbl ? lane & 7 : lane & 3;
        LAS f2* tiof = (LAS f2*)(smem + ((kpx & 4) ? L_TB : L_TA)) + (jj + (jj >> 3)) * 2 + kp * 612;
        LAS f2* rd = TT + w4 * 612 + mp * 144 + mt * 2 + me;
        LAS f2* bxw = TT + w4 * 608 + mp * 130 + me * 8 + mt;
        LAS f2* bxr = TT + w4 * 608 + mp * 130 + me * 8 + mt * 16;
        TwA<8> H;
        TwA<4> tw256; TwA<8> tw64;
        u32 g0 = t0, g1 = t1, g2 = t2, g3 = t3, oe2 = 0;
        const bool s0w = (b < 8 && w == 0) || (b >= 248 && w == 7);
        __builtin_amdgcn_s_setprio(0);
        if (p2act) {
            twfill(tw256, jj, 1.f / 256.f); twfill(tw64, mt, 1.f / 64.f);
            SPIN_LDS(RFL(flag[hiw ? 3 : 1]) == 1);
            const LAS u16* wst = (const LAS u16*)(smem + (hiw ? L_WSTB : L_WST)) + (4 * mt + mp) * 8 + w4 * 2 + me;
            const float sc = 0.5f / (2048.f * 576.f);
#pragma unroll
            for (int d = 0; d < 8; ++d) { const f2 hv = f2{bfl(wst[d * 256]), bfl(wst[2048 + d * 256])} * sc; H.set(d, hv.x, f2{-hv.y, hv.y}); }
            if (s0w) {
                SPIN_LDS(RFL(flag[4]) == 1);
                const bool mine = me == (w == 7);
#pragma unroll
                for (int d = 0; d < 8; ++d) {
                    const f2 sv = *(const LAS f2*)(smem + L_S0 + (32 * d + 4 * mt + mp) * 16);
                    if (mine) H.set(d, sv.x, f2{-sv.y, sv.y});
                }
            }
            H.pin(); PIN1(H.n[0]);
            u32 o = WS_E1 + cls * E1_CLS + e1off(k0, jj) + kpx * 16;
            oe2 = WS_E2 + e2off(k0, jj) + cls * 4608 + kpx * 16;
            PIN1(tiof); PIN1(rd); PIN1(bxw); PIN1(bxr); PIN1(o); PIN1(oe2); PIN1(g0); PIN1(g1); PIN1(g2); PIN1(g3);
            __builtin_amdgcn_s_setprio(2);
            vm0();
            u32x4 g[4];
#pragma unroll 1
            for (int spin = 0; spin < SPIN_CAP; ++spin) {
                CFENCE();
#pragma unroll
                for (int i = 0; i < 4; ++i) g[i] = LD128(o, i * 8192);
                u32 bad = 0;
#pragma unroll
                for (int i = 0; i < 4; ++i) { g[i].x ^= t0; g[i].y ^= t1; g[i].z ^= t2; g[i].w ^= t3; bad |= g[i].x | g[i].y | g[i].z | g[i].w; }
                if (BALLOT((bad & 1023u) != 0) == 0) break;
            }
            f2 a[4], c[4];
#pragma unroll
            for (int i = 0; i < 4; ++i) { a[i] = asf2(g[i].x, g[i].y); c[i] = asf2(g[i].z, g[i].w); }
            r4<1>(a[0], a[1], a[2], a[3]); r4<1>(c[0], c[1], c[2], c[3]);
#pragma unroll
            for (int p = 0; p < 4; ++p) {
                if (p) { a[p] = cmul(a[p], tw256[p]); c[p] = cmul(c[p], tw256[p]); }
                *(LAS f4*)(tiof + p * 144) = f4{a[p].x, a[p].y, c[p].x, c[p].y};
            }
        }
        __builtin_amdgcn_s_setprio(2);
        bar();
        if (p2act) {
            f2 v[8];
#pragma unroll
            for (int i = 0; i < 8; ++i) v[i] = ldz(rd + 18 * i);
            r8<1>(v);
#pragma unroll
            for (int c = 1; c < 8; ++c) v[c] = cmul(v[c], tw64[c]);
            CFENCE();
#pragma unroll
            for (int c = 0; c < 8; ++c) stz(bxw + c * 16, v[c]);
            CFENCE();
#pragma unroll
            for (int t = 0; t < 8; t += 2) { f4 z = *(const LAS f4*)(bxr + t); v[t] = z.xy; v[t + 1] = z.zw; }
            r8<1>(v);
            if (s0w && b < 8 && (cls & 3) == 0) {
                LAS f2* ex = (LAS f2*)(smem + L_S0 + 4096);
                const int q0 = 4 * mt + mp;
                if (me == 0) {
#pragma unroll
                    for (int d = 0; d < 8; ++d) ex[32 * d + q0] = v[d];
                }
                CFENCE();
#pragma unroll
                for (int d = 0; d < 8; ++d) {
                    const int q = 32 * d + q0;
                    const f2 xm = ex[cls == 0 ? (256 - q) & 255 : 255 - q];
                    const f2 hb = *(const LAS f2*)(smem + L_S0 + q * 16 + 8);
                    v[d] = cmul(v[d], H[d]);
                    if (me == 0) v[d] += cmulc(hb, mktw(xm));
                }
            } else {
#pragma unroll
                for (int d = 0; d < 8; ++d) v[d] = cmul(v[d], H[d]);
            }
            r8<-1>(v);
#pragma unroll
            for (int c = 1; c < 8; ++c) v[c] = cmulc(v[c], tw64[c]);
            CFENCE();
#pragma unroll
            for (int c = 0; c < 8; ++c) stz(bxw + c * 16, v[c]);
            CFENCE();
#pragma unroll
            for (int t = 0; t < 8; t += 2) { f4 z = *(const LAS f4*)(bxr + t); v[t] = z.xy; v[t + 1] = z.zw; }
            r8<-1>(v);
            CFENCE();
#pragma unroll
            for (int i = 0; i < 8; ++i) stz(rd + 18 * i, v[i]);
        }
        bar();
        if (p2act) {
            f2 a[4], c[4];
#pragma unroll
            for (int p = 0; p < 4; ++p) {
                f4 z = *(const LAS f4*)(tiof + p * 144);
                a[p] = z.xy; c[p] = z.zw;
                if (p) { a[p] = cmulc(a[p], tw256[p]); c[p] = cmulc(c[p], tw256[p]); }
            }
            r4<-1>(a[0], a[1], a[2], a[3]); r4<-1>(c[0], c[1], c[2], c[3]);
#pragma unroll
            for (int i = 0; i < 4; ++i) ST128((u32x4{tagd(a[i].x, g0), tagd(a[i].y, g1), tagd(c[i].x, g2), tagd(c[i].y, g3)}), oe2 + i * 32 * E2_RS);
        }
        f2 xw[6], zf[8], wf[8];
        float xw_l;
        TwA<6> tv1, tv2;
        const LAS f2* ev = (const LAS f2*)(smem + L_TA) + w4 * 290 + (lane < 36 ? lane : 35);
        const LAS f2* ev4 = ev + (lane == 0 ? 288 : 144);
        LAS f2* zs1 = Zw + (lane & 7) * 36 + (lane >> 3);
        LAS f2* zs2 = Zw + (lane >> 3) * 56 + (lane & 7);
        LAS f2* zl = Zw + lane;
        {
            __builtin_amdgcn_s_setprio(0);
            {
                const LAS f4* t = (const LAS f4*)(smem + L_TW) + l48;
                f4 q[8];
#pragma unroll
                for (int i = 0; i < 8; ++i) q[i] = t[i * 48];
                tv1.xp[0] = q[0].xy; tv1.xp[1] = q[0].zw; tv1.xp[2] = q[1].xy; tv1.n[1] = q[1].zw; tv1.n[2] = q[2].xy; tv1.n[3] = q[2].zw; tv1.n[4] = q[3].xy; tv1.n[5] = q[3].zw;
                tv2.xp[0] = q[4].xy; tv2.xp[1] = q[4].zw; tv2.xp[2] = q[5].xy; tv2.n[1] = q[5].zw; tv2.n[2] = q[6].xy; tv2.n[3] = q[6].zw; tv2.n[4] = q[7].xy; tv2.n[5] = q[7].zw;
                tv1.pin(); tv2.pin();
            }
            const int l36 = lane < 36 ? lane : 35;
#pragma unroll
            for (int c = 0; c < 8; ++c) {
                const int p = (c == 4 && lane == 0) ? 288 : l36 + 36 * c;
                zf[c] = Zw[p]; wf[c] = ((const LAS f2*)(smem + L_WH))[p];
                PIN1(zf[c]); PIN1(wf[c]);
            }
#pragma unroll
            for (int i = 0; i < 6; ++i) { PIN1(xr[i]); xw[i] = unbf(xr[i]); PIN1(xw[i]); }
            {
                f2 pl_ = zf[0] * wf[0], ph_ = zf[4] * wf[4];
#pragma unroll
                for (int c = 1; c < 4; ++c) { pl_ = pfma(zf[c], wf[c], pl_); ph_ = pfma(zf[c + 4], wf[c + 4], ph_); }
                if (lane == 0) pl_ = pfma(zf[0] * wf[0], f2{-0.5f, -0.5f}, pl_);
                pl_ += ph_;
                xw_l = (pl_.x + pl_.y) * (sd1 * (1.f / 288.f));
                PIN1(xw_l);
            }
            PIN1(ev); PIN1(ev4); PIN1(zs1); PIN1(zs2); PIN1(zl);
            __builtin_amdgcn_s_setprio(2);
        }
        auto class_inv = [&](auto HC) {
            constexpr int H = decltype(HC)::value;
            u32x2 gd[4], gm[4];
#pragma unroll 1
            for (int spin = 0; spin < SPIN_CAP; ++spin) {
                CFENCE();
#pragma unroll
                for (int i = 0; i < 4; ++i) { gd[i] = LD64(o2d, (2 * i + H) * 4608); gm[i] = LD64(o2m, (2 * i + H) * 4608); }
                u32 bad = 0;
#pragma unroll
                for (int i = 0; i < 4; ++i) { gd[i].x ^= tdx; gd[i].y ^= tdy; gm[i].x ^= tmx; gm[i].y ^= tmy; bad |= gd[i].x | gd[i].y | gm[i].x | gm[i].y; }
                if (BALLOT((bad & 1023u) != 0) == 0) break;
            }
            announce(6);
            f2 Dv[4], Mv[4], A[4], B[4];
#pragma unroll
            for (int i = 0; i < 4; ++i) { Dv[i] = asf2(gd[i].x, gd[i].y); Mv[i] = asf2(gm[i].x, gm[i].y); }
            if (w == 3 * H) {
                if (H) {
                    const f2 c0 = Dv[0] + Dv[3].yx, c3 = (Dv[0] - Dv[3].yx) * PM, c1 = Dv[1] + Dv[2].yx, c2 = (Dv[1] - Dv[2].yx) * PM;
                    if (lane == 0) { Dv[0] = c0; Dv[1] = c1; Dv[2] = c2; Dv[3] = c3; }
                } else {
                    const f2 c1 = Dv[1] + Dv[3].yx, c3 = (Dv[1] - Dv[3].yx) * PM;
                    if (lane == 0) { Dv[1] = c1; Dv[3] = c3; }
                }
            }
            if (!H) Mv[2] = cmulc(Mv[2], u256);
#pragma unroll
            for (int i = 0; i < 4; ++i) {
                const int ni = H ? 3 - i : (4 - i) & 3;
                f2 e = add_conj(Dv[i], Mv[ni]);
                const f2 t = cmulc(sub_conj(Dv[i], Mv[ni]), wc[i]);
                if (H || i) e = cmulc(e, cw[i]);
                A[i] = add_pi(e, t); B[i] = add_mi(e, t);
            }
            r4<-1>(A[0], A[1], A[2], A[3]); r4<-1>(B[0], B[1], B[2], B[3]);
            if (H) {
                const f2 hh = f2{C_H, C_H};
                A[1] = add_pi(A[1], A[1]) * hh; B[1] = add_pi(B[1], B[1]) * hh;
                A[2] = A[2].yx * MP; B[2] = B[2].yx * MP;
                A[3] = nadd_pi(A[3], A[3]) * hh; B[3] = nadd_pi(B[3], B[3]) * hh;
            }
            LAS f2* zp = (LAS f2*)(smem + L_TA) + H * 4 * 290 + kI;
            LAS f2* zq = (LAS f2*)(smem + L_TA) + H * 4 * 290 + 288 - kI;
#pragma unroll
            for (int a = 0; a < 4; ++a) { zp[a * 290] = A[a]; zq[a * 290] = B[a]; }
        };
        if (w < 6) {
            vm0();
            if (w == 0) flag[6] = 1;
            else if (!p2act) SPIN_LDS(RFL(flag[6]) == 1);
            if (lane < 48) { if (h) class_inv(std::integral_constant<int, 1>{}); else class_inv(std::integral_constant<int, 0>{}); }
        }
        __builtin_amdgcn_s_setprio(3);
        bar();
        u32 po[6], ph[6], pl[6];
        auto reprio = [&]() {
            const u32 kk = ((volatile LAS u32*)(smem + L_KEYS))[lane & 7];
            if (__builtin_popcountll(BALLOT(kk > (u32)__builtin_amdgcn_readlane((int)kk, w)) & 0xFFull) < 2) __builtin_amdgcn_s_setprio(2); else __builtin_amdgcn_s_setprio(0);
        };
        {
            f2 v[8];
            {
                const f2 sg = hiw ? f2{-1.f, -1.f} : f2{1.f, 1.f};
                f2 e[8], o[8];
#pragma unroll
                for (int c = 0; c < 8; ++c) { e[c] = ldz(c == 4 ? ev4 : ev + 36 * c); o[c] = ldz(c == 4 ? ev4 + 4 * 290 : ev + 4 * 290 + 36 * c); }
#pragma unroll
                for (int c = 0; c < 8; ++c) v[c] = pfma(o[c], sg, e[c]);
                f2 al = v[0] * zf[0], ah = v[4] * zf[4], bl = v[0] * wf[0], bh = v[4] * wf[4];
#pragma unroll
                for (int c = 1; c < 4; ++c) { al = pfma(v[c], zf[c], al); ah = pfma(v[c + 4], zf[c + 4], ah); bl = pfma(v[c], wf[c], bl); bh = pfma(v[c + 4], wf[c + 4], bh); }
                const float sy = rdl(v[0].x, 0) * (2.f * 288.f / 576.f);
                const float vb = fmaf(dm1 + dm1 - sy, sy, var1), sd2 = sd1 * (2.f / 576.f);
                const float sw = ((rbf(mu1 + sy) - mu1) - dm1) * rdl(wf[0].x, 0);
                const f2 z0 = sumdiff(v[0]), z4 = v[4] * PM;
                if (lane == 0) { v[0] = z0; v[4] = z4; }
                f2 q0 = v[0] * v[0], q1 = v[1] * v[1];
#pragma unroll
                for (int c = 2; c < 8; c += 2) { q0 = pfma(v[c], v[c], q0); q1 = pfma(v[c + 1], v[c + 1], q1); }
                al = pfma(ah, PM, al); bl = pfma(bh, PM, bl); q0 += q1;
                const bool act = lane < 36;
                const float t = rowsum(sw16(sw32(act ? al.x + al.y : 0.f, act ? q0.x + q0.y : 0.f), sw32(act ? bl.x + bl.y + xw_l : 0.f, 0.f)));
                const float sly = rdl(t, 0), swy = rdl(t, 16), syy = rdl(t, 32);
                const float varh = fmaxf(fmaf(syy, 0.5f, fmaf(sly, sd2, vb)), 0.f);
                const u32 bits = pkbf(f2{(swy - sw) * __builtin_amdgcn_rcpf(rbf(__builtin_amdgcn_sqrtf(rbf(rbf(varh) + 1e-5f)))), 0.f}) & 0xFFFFu;
                u32 key = (bits & 0x8000u) ? (bits ^ 0xFFFFu) : (bits | 0x8000u);
                if ((bits & 0x7FFFu) > 0x7F80u) key = 0;
                ((volatile LAS u32*)(smem + L_KEYS))[w] = (key << 16) | (0xFFFFu - row);
                __builtin_amdgcn_s_setprio(1);
#pragma unroll
                for (int c = 0; c < 8; ++c) PIN1(v[c]);
                f2 a0 = add_conj(v[0], v[4]), a4 = sub_conj(v[0], v[4]), a1 = add_conj(v[1], v[5]), a5 = sub_conj(v[1], v[5]);
                f2 a2 = add_conj(v[2], v[6]), a6 = sub_conj(v[2], v[6]), a3 = add_conj(v[3], v[7]), a7 = sub_conj(v[3], v[7]);
                r4<-1>(a0, a1, a2, a3); r4odd<-1>(a4, a5, a6, a7);
                if (act) {
                    zl[0 * 36] = a0; zl[2 * 36] = a1; zl[4 * 36] = a2; zl[6 * 36] = a3;
                    zl[1 * 36] = a4; zl[3 * 36] = a5; zl[5 * 36] = a6; zl[7 * 36] = a7;
                }
                CFENCE();
                if (lane < 48) {
#pragma unroll
                    for (int i = 0; i < 6; ++i) v[i] = ldz(zs1 + 6 * i);
                }
                reprio();
            }
            if (lane < 48) {
#pragma unroll
                for (int i = 1; i < 6; ++i) v[i] = cmulc(v[i], tv2[i]);
                r6<-1>(v);
                CFENCE();
#pragma unroll
                for (int i = 0; i < 6; ++i) stz(zs2 + 8 * i, v[i]);
                reprio();
                CFENCE();
#pragma unroll
                for (int i = 0; i < 6; ++i) v[i] = ldz(zl + i * 56);
#pragma unroll
                for (int i = 1; i < 6; ++i) v[i] = cmulc(v[i], tv1[i]);
                r6<-1>(v);
            }
#pragma unroll
            for (int i = 0; i < 6; ++i) { ph[i] = pkbf(xw[i] + rbf2(v[i])); v[i] = unbf(ph[i]); }
            f2 d[6];
#pragma unroll
            for (int i = 0; i < 6; ++i) d[i] = v[i] - mu1;
            const f2 a1 = (d[0] + d[1]) + (d[2] + d[3]) + (d[4] + d[5]);
            const f2 a2 = pfma(d[0], d[0], pfma(d[2], d[2], d[4] * d[4])) + pfma(d[1], d[1], pfma(d[3], d[3], d[5] * d[5]));
            const float e = sw32(lane < 48 ? a1.x + a1.y : 0.f, lane < 48 ? a2.x + a2.y : 0.f);
            u32 kk = ((volatile LAS u32*)(smem + L_KEYS))[lane & 7];
            const float t = rowsum(sw16(e, e));
            const float md = rdl(t, 0) * (1.f / 576.f);
            const float var = fmaxf(fmaf(-md, md, rdl(t, 32) * (1.f / 576.f)), 0.f);
            const f2 mv = rbf2(f2{mu1 + md, var});
            const float rsd = __builtin_amdgcn_rcpf(rbf(__builtin_amdgcn_sqrtf(rbf(mv.y + 1e-5f))));
#pragma unroll
            for (int i = 0; i < 6; ++i) pl[i] = pkbf(rbf2(v[i] - mv.x) * rsd);
            if (__builtin_expect(BALLOT(kk == 0) != 0, 0)) { SPIN_LDS(BALLOT(((volatile LAS u32*)(smem + L_KEYS))[lane & 7] == 0) == 0); kk = ((volatile LAS u32*)(smem + L_KEYS))[lane & 7]; }
            const u32 rank = __builtin_popcountll(BALLOT(kk > (u32)__builtin_amdgcn_readlane((int)kk, w)) & 0xFFull);
            if (rank < 2 && lane < 48) {
                const u32 ro = WS_ROWL + b * 3072 + rank * 1536 + lane * 32;
                ST128((u32x4{pl[0], pl[1], pl[2], tagA}), ro); ST128((u32x4{pl[3], pl[4], pl[5], tagA}), ro + 16);
            }
#pragma unroll
            for (int i = 0; i < 6; ++i) po[i] = pkbf(v[i] + unbf(pl[i]));
        }
        auto publish_row = [&](const u32* pk, u32 off) {
            if (lane < 48) { ST128((u32x4{pk[0], pk[1], pk[2], tagA}), off + lane * 32); ST128((u32x4{pk[3], pk[4], pk[5], tagA}), off + lane * 32 + 16); }
        };
        auto out_rows = [&]() {
            const u32x2 top = *(const LAS u32x2*)(smem + L_TOP);
            const u32 row1 = (0xFFFFu - (RFL(top.x) & 0xFFFFu)), row2 = (0xFFFFu - (RFL(top.y) & 0xFFFFu));
            if (row == row1) publish_row(ph, WS_ROWH);
            else if (row == row2) publish_row(ph, WS_ROWH + 1536);
            else if (lane < 48) {
                u32* o = (u32*)(out + (size_t)row * 576) + lane;
#pragma unroll
                for (int i = 0; i < 6; ++i) o[48 * i] = po[i];
            }
        };
        if (moe) {
            if (!hiw) {
                s16x4 fa[36];
                LAS char* mb0 = (LAS char*)(smem + L_BIG) + w4 * 32 + tp * 8 + (8 * tg + tq) * 128;
                LAS char* mb1 = mb0 + 9 * 4096;
                PIN1(mb0); PIN1(mb1);
                SPIN_LDS(BALLOT(flag[8 + (lane & 3)] != 1 && (lane & 3) != 2) == 0);
#pragma unroll
                for (int s = 0; s < 36; ++s)
                    fa[s] = __builtin_bit_cast(s16x4, __builtin_amdgcn_ds_read_tr16_b64_v4i16((LAS s16x4*)((s < 18 ? mb0 : mb1) + (s % 18) / 2 * 4096 + (s & 1) * 512)));
#pragma unroll
                for (int i = 0; i < 36; ++i) PIN1(fa[i]);
                const LAS char* xs = (const LAS char*)(smem + L_XS) + (lane & 1) * 1152 + tg * 16;
                u32 og = WS_GU + (b - 80) * 1024 + ((lane & 1) * 16 + w * 4 + tg) * 16;
                PIN1(xs); PIN1(og);
                __builtin_amdgcn_s_setprio(3);
                bar();
                f4 acc = {0.f, 0.f, 0.f, 0.f}, acc2 = {0.f, 0.f, 0.f, 0.f};
                bf8 ba[6], bb[6];
                auto mm = [&](int st, bf8 bv) {
                    const s16x8 af = __builtin_shufflevector(fa[2 * st], fa[2 * st + 1], 0, 1, 2, 3, 4, 5, 6, 7);
                    if (st & 1) acc2 = __builtin_amdgcn_mfma_f32_16x16x32_bf16(__builtin_bit_cast(bf8, af), bv, acc2, 0, 0, 0);
                    else acc = __builtin_amdgcn_mfma_f32_16x16x32_bf16(__builtin_bit_cast(bf8, af), bv, acc, 0, 0, 0);
                };
#pragma unroll
                for (int i = 0; i < 6; ++i) ba[i] = *(const LAS bf8*)(xs + i * 64);
#pragma unroll
                for (int i = 0; i < 6; ++i) bb[i] = *(const LAS bf8*)(xs + (6 + i) * 64);
                __builtin_amdgcn_sched_barrier(0);
#pragma unroll
                for (int i = 0; i < 6; ++i) mm(i, ba[i]);
                __builtin_amdgcn_sched_barrier(0);
#pragma unroll
                for (int i = 0; i < 6; ++i) ba[i] = *(const LAS bf8*)(xs + (12 + i) * 64);
                __builtin_amdgcn_sched_barrier(0);
#pragma unroll
                for (int i = 0; i < 6; ++i) mm(6 + i, bb[i]);
                __builtin_amdgcn_sched_barrier(0);
#pragma unroll
                for (int i = 0; i < 6; ++i) mm(12 + i, ba[i]);
                acc += acc2;
                f2 ga = rbf2(acc.xy), gb = rbf2(acc.zw);
                if (b < 152) {
                    const f2 ea = ga * -1.4426950408889634f, eb = gb * -1.4426950408889634f;
                    ga = ga * f2{__builtin_amdgcn_rcpf(1.f + __builtin_amdgcn_exp2f(ea.x)), __builtin_amdgcn_rcpf(1.f + __builtin_amdgcn_exp2f(ea.y))};
                    gb = gb * f2{__builtin_amdgcn_rcpf(1.f + __builtin_amdgcn_exp2f(eb.x)), __builtin_amdgcn_rcpf(1.f + __builtin_amdgcn_exp2f(eb.y))};
                }
                if ((lane & 15) < 2) ST128((u32x4{pkbf(ga), pkbf(gb), tagA, tagB}), og);
            } else bar();
            out_rows();
        } else if (qb) {
            s16x4 fd[20];
            LAS char* db0 = (LAS char*)(smem + L_BIG) + (8 * tg + tq) * 1152 + w * 32 + tp * 8;
            LAS char* db1 = db0 + (32 + w4 - w) * 32;
            PIN1(db0); PIN1(db1);
            SPIN_LDS(BALLOT(flag[8 + (lane & 3)] != 1 && (lane & 3) != 2) == 0);
#pragma unroll
            for (int i = 0; i < 5; ++i)
#pragma unroll
                for (int t = 0; t < 4; ++t)
                    fd[i * 4 + t] = __builtin_bit_cast(s16x4, __builtin_amdgcn_ds_read_tr16_b64_v4i16((LAS s16x4*)((i < 4 ? db0 : db1) + (32 * (t >> 1) + 4 * (t & 1)) * 1152 + (i & 3) * 256)));
#pragma unroll
            for (int i = 0; i < 20; ++i) PIN1(fd[i]);
            u32 opb = WS_PB + (w * 72 + (b - 8)) * 128 + (lane & 1) * 64 + tg * 16;
            PIN1(opb);
            bar();
            out_rows();
            __builtin_amdgcn_s_setprio(3);
            bar();
            const bf8 b0 = *(const LAS bf8*)(smem + L_AB + (lane & 1) * 128 + tg * 16), b1 = *(const LAS bf8*)(smem + L_AB + (lane & 1) * 128 + 64 + tg * 16);
            f4 d[5];
#pragma unroll
            for (int i = 0; i < 5; ++i) {
                const s16x8 a0 = __builtin_shufflevector(fd[4 * i], fd[4 * i + 1], 0, 1, 2, 3, 4, 5, 6, 7);
                d[i] = __builtin_amdgcn_mfma_f32_16x16x32_bf16(__builtin_bit_cast(bf8, a0), b0, f4{0.f, 0.f, 0.f, 0.f}, 0, 0, 0);
            }
#pragma unroll
            for (int i = 0; i < 5; ++i) {
                const s16x8 a1 = __builtin_shufflevector(fd[4 * i + 2], fd[4 * i + 3], 0, 1, 2, 3, 4, 5, 6, 7);
                d[i] = __builtin_amdgcn_mfma_f32_16x16x32_bf16(__builtin_bit_cast(bf8, a1), b1, d[i], 0, 0, 0);
            }
            if ((lane & 15) < 2) {
#pragma unroll
                for (int i = 0; i < 5; ++i) {
                    if (i == 4 && hiw) break;
                    ST128((u32x4{tagd(d[i].x, g0), tagd(d[i].y, g1), tagd(d[i].z, g2), tagd(d[i].w, g3)}), opb + i * (8 * 9216));
                }
            }
        } else {
            bar();
            out_rows();
        }
    }
}

extern "C" void kernel_launch(void* const* d_in, const int* in_sizes, int n_in, void* d_out, int out_size, void* d_ws, size_t ws_size,
                              hipStream_t stream) {
    (void)in_sizes; (void)n_in; (void)out_size;
    (void)ws_size;
    (void)hipFuncSetAttribute((const void*)fused_kernel, hipFuncAttributeMaxDynamicSharedMemorySize, L_TOTAL);
    fused_kernel<<<256, 768, L_TOTAL, stream>>>((const u16*)d_in[0], (const u16*)d_in[1], (const u16*)d_in[2], (const u16*)d_in[4],
                                               (const u16*)d_in[5], (const u16*)d_in[6], (const u16*)d_in[7], (u16*)d_out, (char*)d_ws,
                                               (const u64*)d_ws);
}
```
